```python
import torch

def _softmax(x, axis=-1):
    return torch.softmax(x, dim=axis)
def _gelu(x):
    return torch.nn.functional.gelu(x, approximate="tanh")
def _silu(x):
    return torch.nn.functional.silu(x)
def _sigmoid(x):
    return torch.sigmoid(x)
def _layernorm(x, axis=-1, eps=1e-5):
    m = x.mean(dim=axis, keepdim=True)
    v = x.var(dim=axis, keepdim=True, unbiased=False)
    return (x - m) / torch.sqrt(v + eps)
def _scatter_add(tgt, idx, src, axis=0):
    return tgt.clone().index_add_(axis, idx, src)
def _segment_sum(src, seg, n):
    out = torch.zeros((n,) + src.shape[1:], dtype=src.dtype)
    return out.index_add_(0, seg, src)
def _pad(x, pw):
    flat = tuple(v for pair in reversed(pw) for v in pair)
    return torch.nn.functional.pad(x, flat)

kernel_name = 'parallel_sum__mha_block__dense_activation_relu__prompt'

def setup_inputs():

    _w = (lambda a: (lambda t: t.to(torch.bfloat16) if t.is_floating_point() else t)(torch.as_tensor(a)))
    def _A_setup():
        B, S, D, H = 1, 2048, 448, 16
        return {
            "x": _w((lambda *s: torch.randn(*s, dtype=torch.bfloat16) * 0.05)(B, S, D)),
            "wqkv": _w((lambda *s: torch.randn(*s, dtype=torch.bfloat16) * 0.2)(D, 3 * D)),
            "wo": _w((lambda *s: torch.randn(*s, dtype=torch.bfloat16) * 0.1)(D, D)),
        }
    _A_in = _A_setup()

    def _B_setup():
        B, S, D, Dff = 1, 2048, 448, 1792
        return {
            "x": _w((lambda *s: torch.randn(*s, dtype=torch.bfloat16) * 0.05)(B, S, D)),
            "w1": _w((lambda *s: torch.randn(*s, dtype=torch.bfloat16) * 0.2)(D, Dff)),
            "w2": _w((lambda *s: torch.randn(*s, dtype=torch.bfloat16) * 0.2)(Dff, D)),
        }
    _B_in = _B_setup()

    return {
        "A_x": _A_in["x"],
        "A_wqkv": _A_in["wqkv"],
        "A_wo": _A_in["wo"],
        "B_x": _B_in["x"],
        "B_w1": _B_in["w1"],
        "B_w2": _B_in["w2"],
    }

def reference(A_x, A_wqkv, A_wo, B_x, B_w1, B_w2):
    def _A_ref(x, wqkv, wo):
        B, S, D = x.shape
        H = 16
        Dh = D // H
        qkv = (lambda x, *d: x.permute(*d))((x @ wqkv).reshape(B, S, 3, H, Dh), 2, 0, 3, 1, 4)
        q, k, v = qkv[0], qkv[1], qkv[2]
        scores = torch.einsum("bhqd,bhkd->bhqk", q, k) * (Dh ** -0.5)
        p = _softmax(scores, axis=-1)
        o = torch.einsum("bhqk,bhkd->bhqd", p, v)
        return (lambda x, *d: x.permute(*d))(o, 0, 2, 1, 3).reshape(B, S, D) @ wo

    def _B_ref(x, w1, w2):
        h = torch.einsum("bsd,df->bsf", x, w1)
        zero = torch.zeros_like(h)
        h = torch.maximum(h, zero)
        return torch.einsum("bsf,fd->bsd", h, w2)

    x = A_x
    xn = _layernorm(x, axis=-1)
    return x + _A_ref(xn, A_wqkv, A_wo) + _B_ref(xn, B_w1, B_w2)
```

```cpp
#include <hip/hip_runtime.h>
#include <hip/hip_bf16.h>
typedef __hip_bfloat16 bf16;
constexpr int S=2048, D=448, H=16, DH=28, F=1792;
__device__ inline float b2f(bf16 v){ return __bfloat162float(v); }
__global__ void k_ln(const bf16* x, float* xn){
  int r = blockIdx.x; int l = threadIdx.x;
  float s=0; for(int i=l;i<D;i+=64) s+=b2f(x[r*D+i]);
  for(int o=32;o;o>>=1) s+=__shfl_xor(s,o);
  float m=s/D; float v=0; for(int i=l;i<D;i+=64){ float d=b2f(x[r*D+i])-m; v+=d*d; }
  for(int o=32;o;o>>=1) v+=__shfl_xor(v,o);
  float rs=rsqrtf(v/D+1e-5f);
  for(int i=l;i<D;i+=64) xn[r*D+i]=b2f(__float2bfloat16((b2f(x[r*D+i])-m)*rs));
}
__global__ void k_gemm(const float* A, const bf16* B, float* C, int M, int N, int K, int relu){
  __shared__ float As[16][17], Bs[16][17];
  int tx=threadIdx.x&15, ty=threadIdx.x>>4; int r=blockIdx.y*16+ty, c=blockIdx.x*16+tx;
  float acc=0;
  for(int k0=0;k0<K;k0+=16){
    As[ty][tx]=A[r*K+k0+tx]; Bs[ty][tx]=b2f(B[(k0+ty)*N+c]);
    __syncthreads();
    for(int k=0;k<16;++k) acc+=As[ty][k]*Bs[k][tx];
    __syncthreads();
  }
  if(relu) acc=fmaxf(acc,0.f);
  C[r*N+c]=b2f(__float2bfloat16(acc));
}
__global__ void k_attn(const float* qkv, float* o){
  int h=blockIdx.y; int q=blockIdx.x*64+threadIdx.x;
  float qv[DH]; for(int d=0;d<DH;++d) qv[d]=qkv[q*3*D+h*DH+d];
  float m=-1e30f,l=0; float acc[DH]; for(int d=0;d<DH;++d) acc[d]=0;
  const float sc=rsqrtf((float)DH);
  for(int k=0;k<S;++k){
    float s=0; for(int d=0;d<DH;++d) s+=qv[d]*qkv[k*3*D+D+h*DH+d];
    s*=sc; float mn=fmaxf(m,s); float a=__expf(m-mn), p=__expf(s-mn);
    l=l*a+p; for(int d=0;d<DH;++d) acc[d]=acc[d]*a+p*qkv[k*3*D+2*D+h*DH+d];
    m=mn;
  }
  for(int d=0;d<DH;++d) o[q*D+h*DH+d]=b2f(__float2bfloat16(acc[d]/l));
}
__global__ void k_fin(const bf16* x, const float* a, const float* f, bf16* out){
  int i=blockIdx.x*256+threadIdx.x; out[i]=__float2bfloat16(b2f(__float2bfloat16(b2f(x[i])+a[i]))+f[i]);
}
extern "C" void kernel_launch(void* const* d_in, const int* in_sizes, int n_in, void* d_out, int out_size, void* d_ws, size_t ws_size, hipStream_t stream){
  const bf16* x=(const bf16*)d_in[0]; const bf16* wqkv=(const bf16*)d_in[1]; const bf16* wo=(const bf16*)d_in[2];
  const bf16* w1=(const bf16*)d_in[4]; const bf16* w2=(const bf16*)d_in[5];
  float* xn=(float*)d_ws; float* qkv=xn+S*D; float* o=qkv+S*3*D; float* a=o+S*D; float* hh=a+S*D; float* f=hh+S*F;
  k_ln<<<S,64,0,stream>>>(x,xn);
  k_gemm<<<dim3(3*D/16,S/16),256,0,stream>>>(xn,wqkv,qkv,S,3*D,D,0);
  k_attn<<<dim3(S/64,H),64,0,stream>>>(qkv,o);
  k_gemm<<<dim3(D/16,S/16),256,0,stream>>>(o,wo,a,S,D,D,0);
  k_gemm<<<dim3(F/16,S/16),256,0,stream>>>(xn,w1,hh,S,F,D,1);
  k_gemm<<<dim3(D/16,S/16),256,0,stream>>>(hh,w2,f,S,D,F,0);
  k_fin<<<S*D/256,256,0,stream>>>(x,a,f,(bf16*)d_out);
}
```

```cpp
#include <hip/hip_runtime.h>
#include <hip/hip_bf16.h>
#include <stdint.h>

typedef short bf16x8 __attribute__((ext_vector_type(8)));
typedef short s16x4 __attribute__((ext_vector_type(4)));
typedef float f32x16 __attribute__((ext_vector_type(16)));
typedef float f32x4 __attribute__((ext_vector_type(4)));
typedef unsigned u32x4 __attribute__((ext_vector_type(4)));
typedef __bf16 bf16x2 __attribute__((ext_vector_type(2)));
typedef unsigned long long u64;
#define GAS __attribute__((address_space(1)))
#define LAS __attribute__((address_space(3)))
#define DI __device__ __forceinline__
#define MFMA(a, b, c) __builtin_amdgcn_mfma_f32_32x32x16_bf16((a), (b), (c), 0, 0, 0)
#define SBAR() __builtin_amdgcn_sched_barrier(0)
#define PIN(x) asm volatile("" : "+v"(x))
#define RLX_AGENT __ATOMIC_RELAXED, __HIP_MEMORY_SCOPE_AGENT
#define WAITV(n) asm volatile("s_waitcnt vmcnt(" #n ")" ::: "memory")
#define BARRIER() asm volatile("s_barrier" ::: "memory")
#define BARRIER_L() asm volatile("s_waitcnt lgkmcnt(0)\n\ts_barrier" ::: "memory")

constexpr int S = 2048, D = 448, H = 16, DH = 28, F = 1792;
constexpr int XP = D * 2;
constexpr int WQP = 3 * D * 2;
constexpr int W1P = F * 2;
constexpr float QSCALE = 0.18898223650461362f * 1.4426950408889634f;
constexpr float SHIFT = 60.f;

constexpr int WS_EPOCH = 0;
constexpr int WS_FQ = 4096;
constexpr int WS_FH = WS_FQ + 256 * 128;
constexpr int WS_FO = WS_FH + 256 * 128;
constexpr int WS_PROF = 128 * 1024;
constexpr int WS_QKV = 1 << 20;
constexpr int WS_H = WS_QKV + 48 * 64 * 2048;
constexpr int WS_O = WS_H + 64 * 56 * 2048;
constexpr int WS_END = WS_O + 64 * 16 * 2048;

constexpr int L_RING = 0;
constexpr int L_XS = 131072;
constexpr int L_G2 = 0;
constexpr int L_MRG = 98304;
constexpr int L_MST = 147456;
constexpr int L_CNT = L_MST + 4096;
constexpr int LDS_BYTES = 163840;
constexpr int L_RSTD = L_CNT + 64;
#define PUBP 0
#define XDELAY 16
constexpr unsigned SPIN_CAP = 1u << 18;

#define PROF_ARG
#define PROF_PASS
#define STAMP(i) do {} while (0)

DI unsigned pk2(float lo, float hi) { bf16x2 r = {(__bf16)lo, (__bf16)hi}; return __builtin_bit_cast(unsigned, r); }
DI float rbf(float v) { return (float)(__bf16)v; }
DI float dot2(unsigned a, unsigned b, float c) { return __builtin_amdgcn_fdot2_f32_bf16(__builtin_bit_cast(bf16x2, a), __builtin_bit_cast(bf16x2, b), c, false); }
template <int CTRL> DI float dpp(float x) { return __builtin_bit_cast(float, __builtin_amdgcn_mov_dpp(__builtin_bit_cast(int, x), CTRL, 0xf, 0xf, true)); }
DI float sum8(float v) { v += dpp<0xB1>(v); v += dpp<0x4E>(v); v += dpp<0x141>(v); return v; }
DI float xhalf_max(float m) { auto r = __builtin_amdgcn_permlane32_swap(__float_as_uint(m), __float_as_uint(m), false, false); return fmaxf(__uint_as_float(r[0]), __uint_as_float(r[1])); }
DI float hi_half(float m) { auto r = __builtin_amdgcn_permlane32_swap(__float_as_uint(m), __float_as_uint(m), false, false); return __uint_as_float(r[1]); }
DI s16x4 trd(LAS const char* p) { typedef short v4s __attribute__((ext_vector_type(4))); return __builtin_bit_cast(s16x4, __builtin_amdgcn_ds_read_tr16_b64_v4i16((LAS v4s*)p)); }
DI bf16x8 cat(s16x4 a, s16x4 b) { return __builtin_shufflevector(a, b, 0, 1, 2, 3, 4, 5, 6, 7); }
DI void glds(const void* sbase, unsigned voff, unsigned lds_dst) {
  unsigned keep;
  asm volatile("s_mov_b32 %0, m0\n\ts_mov_b32 m0, %3\n\ts_nop 0\n\tglobal_load_lds_dwordx4 %1, %2\n\ts_mov_b32 m0, %0"
               : "=&s"(keep) : "v"(voff), "s"(sbase), "s"(lds_dst) : "memory");
}
DI unsigned lds_arrive(LAS char* lds, int idx) {
  unsigned r = 0;
  asm volatile("s_waitcnt lgkmcnt(0)" ::: "memory");
  if ((threadIdx.x & 63) == 0) r = __hip_atomic_fetch_add((LAS unsigned*)(lds + L_CNT) + idx, 1u, __ATOMIC_RELAXED, __HIP_MEMORY_SCOPE_WORKGROUP);
  return __builtin_amdgcn_readfirstlane(r);
}
DI void lds_wait(LAS char* lds, int idx, unsigned n) {
#pragma unroll 1
  for (unsigned sp = 0; sp < SPIN_CAP; ++sp) {
    unsigned v = __builtin_amdgcn_readfirstlane(__hip_atomic_load((LAS unsigned*)(lds + L_CNT) + idx, __ATOMIC_RELAXED, __HIP_MEMORY_SCOPE_WORKGROUP));
    if (v >= n) break;
    __builtin_amdgcn_s_sleep(1);
  }
  asm volatile("" ::: "memory");
}
DI void poll_flags(char* ws, unsigned off, u64 tag) {
  GAS u64* p = (GAS u64*)(ws + off);
#pragma unroll 1
  for (unsigned sp = 0; sp < SPIN_CAP; ++sp) {
    u64 v = __hip_atomic_load(p, RLX_AGENT);
    if (__all(v == tag)) break;
    __builtin_amdgcn_s_sleep(2);
  }
  __builtin_amdgcn_fence(__ATOMIC_ACQUIRE, "wavefront");
  asm volatile("" ::: "memory");
}
DI void publish(LAS char* lds, int cnt, unsigned n, char* ws, unsigned off, u64 tag) {
  WAITV(0);
  unsigned old = lds_arrive(lds, cnt);
  if (old == n - 1 && (threadIdx.x & 63) == 0) __hip_atomic_store((GAS u64*)(ws + off), tag, RLX_AGENT);
}
DI u32x4 pack8(const f32x16& a, int s) {
  return (u32x4){pk2(a[8 * s], a[8 * s + 1]), pk2(a[8 * s + 2], a[8 * s + 3]), pk2(a[8 * s + 4], a[8 * s + 5]), pk2(a[8 * s + 6], a[8 * s + 7])};
}
#define LD_DS(rsrc, voff, soff) __builtin_bit_cast(bf16x8, __builtin_amdgcn_raw_buffer_load_b128((rsrc), (voff), (soff), 16))
#define ST_WT(v, rsrc, off) __builtin_amdgcn_raw_buffer_store_b128((v), (rsrc), (off), 0, 16)

enum { C_QKV = 0, C_H = 1, C_GDONE = 2, C_MRG = 3, C_O = 4, C_RED = 5 };

DI u32x4 center8(u32x4 w, float negm) {
  float t0, t1, t2, t3, t4, t5, t6, t7; u32x4 r;
  asm("v_dot2_f32_bf16 %4, %12, %17, %16\n\tv_dot2_f32_bf16 %5, %12, %18, %16\n\t"
      "v_dot2_f32_bf16 %6, %13, %17, %16\n\tv_dot2_f32_bf16 %7, %13, %18, %16\n\t"
      "v_dot2_f32_bf16 %8, %14, %17, %16\n\tv_dot2_f32_bf16 %9, %14, %18, %16\n\t"
      "v_dot2_f32_bf16 %10, %15, %17, %16\n\tv_dot2_f32_bf16 %11, %15, %18, %16\n\t"
      "v_cvt_pk_bf16_f32 %0, %4, %5\n\tv_cvt_pk_bf16_f32 %1, %6, %7\n\tv_cvt_pk_bf16_f32 %2, %8, %9\n\tv_cvt_pk_bf16_f32 %3, %10, %11"
      : "=&v"(r[0]), "=&v"(r[1]), "=&v"(r[2]), "=&v"(r[3]), "=&v"(t0), "=&v"(t1), "=&v"(t2), "=&v"(t3), "=&v"(t4), "=&v"(t5), "=&v"(t6), "=&v"(t7)
      : "v"(w[0]), "v"(w[1]), "v"(w[2]), "v"(w[3]), "v"(negm), "s"(0x00003f80u), "s"(0x3f800000u));
  return r;
}
DI u64 mk_tag(u64 e) { e += 1; return ((u64)(unsigned)__builtin_amdgcn_readfirstlane((unsigned)(e >> 32)) << 32) | (unsigned)__builtin_amdgcn_readfirstlane((unsigned)e); }
DI s16x4 trd_a(unsigned a) { return trd((LAS const char*)(uintptr_t)a); }
#define OPAQUE(x) asm volatile("" : "+v"(x))

DI void qkv_out(f32x16 a, int b, float rstd, LAS char* xs, __amdgpu_buffer_rsrc_t rs, int rb, int lane) {
  const int r = lane & 31, hh = lane >> 5;
  const float sc = b < 16 ? rstd * QSCALE : rstd;
#pragma unroll
  for (int e = 0; e < 16; ++e) a[e] *= sc;
  if (hh) { a[12] = b < 16 ? 0.f : 1.f; a[13] = (b >= 16 && b < 32) ? 1.f : 0.f; a[14] = 0.f; a[15] = 0.f; }
  u32x4 f0 = pack8(a, 0), f1 = pack8(a, 1);
  if (b >= 32) {
    typedef unsigned u32x2 __attribute__((ext_vector_type(2)));
    LAS char* im = xs + 2048;
    LAS char* wp = im + r * 64 + hh * 8;
    *(LAS u32x2*)(wp) = (u32x2){f0[0], f0[1]}; *(LAS u32x2*)(wp + 16) = (u32x2){f0[2], f0[3]};
    *(LAS u32x2*)(wp + 32) = (u32x2){f1[0], f1[1]}; *(LAS u32x2*)(wp + 48) = (u32x2){f1[2], f1[3]};
    LAS const char* rp = im + (4 * hh + ((lane & 15) >> 2)) * 64 + ((lane >> 4) & 1) * 32 + (lane & 3) * 8;
    f0 = __builtin_bit_cast(u32x4, cat(trd(rp), trd(rp + 512)));
    f1 = __builtin_bit_cast(u32x4, cat(trd(rp + 1024), trd(rp + 1536)));
  }
  const unsigned o = WS_QKV + (b * 64 + rb) * 2048 + lane * 16;
  ST_WT(f0, rs, o); ST_WT(f1, rs, o + 1024);
}

DI void g_phase1(LAS char* lds, unsigned ldsb, const char* x, char* ws, int rt, int j, int w, int lane, u64 epoch PROF_ARG) {
  const int row0 = rt * 128 + w * 32, rb = rt * 4 + w;
  const int r = lane & 31, hh = lane >> 5;
  LAS char* xs = lds + L_XS + w * 4096;
  bf16x8 xf[28];
  float rstd;
  {
    u32x4 xr[7][4];
    const char* xb = x + (size_t)(row0 + (lane >> 3)) * XP + (lane & 7) * 16;
#pragma unroll
    for (int p = 0; p < 7; ++p)
#pragma unroll
      for (int i = 0; i < 4; ++i) xr[p][i] = *(const u32x4*)(xb + i * 8 * XP + p * 128);
    SBAR();
    float sm[4] = {}, sq[4] = {};
#pragma unroll
    for (int p = 0; p < 7; ++p) {
#pragma unroll
      for (int d = 0; d < 4; ++d)
#pragma unroll
        for (int i = 0; i < 4; ++i) { sm[i] = dot2(xr[p][i][d], 0x3f803f80u, sm[i]); sq[i] = dot2(xr[p][i][d], xr[p][i][d], sq[i]); }
      SBAR();
    }
    STAMP(1);
    float negm[4];
    LAS float* rsl = (LAS float*)(lds + L_RSTD + w * 128);
#pragma unroll
    for (int i = 0; i < 4; ++i) {
      const float mean = sum8(sm[i]) * (1.0f / D);
      negm[i] = -rbf(mean);
      float v = sum8(sq[i]) * (1.0f / D) - mean * mean;
      v = rbf(fmaxf(v, 0.f));
      const float sd = rbf(__builtin_sqrtf(rbf(v + 1e-5f)));
      if ((lane & 7) == 0) rsl[i * 8 + (lane >> 3)] = __builtin_amdgcn_rcpf(sd);
    }
    STAMP(16);
    LAS char* xs_w0 = xs + (lane >> 3) * 128 + (((lane & 7) ^ ((lane >> 4) & 3)) * 16);
    LAS char* xs_w1 = xs + (lane >> 3) * 128 + (((lane & 7) ^ ((lane >> 4) & 3) ^ 4) * 16);
#pragma unroll
    for (int p = 0; p < 7; ++p) {
#pragma unroll
      for (int i = 0; i < 4; ++i) *(LAS u32x4*)(((i & 1) ? xs_w1 : xs_w0) + i * 1024) = center8(xr[p][i], negm[i]);
#pragma unroll
      for (int s = 0; s < 4; ++s) xf[4 * p + s] = *(LAS const bf16x8*)(xs + r * 128 + (((2 * s + hh) ^ ((r >> 1) & 7)) * 16));
      if (p == 2) STAMP(17);
    }
    rstd = rsl[r];
  }
  const u64 tag = mk_tag(epoch);
  const __amdgpu_buffer_rsrc_t rs = __builtin_amdgcn_make_buffer_rsrc(ws, 0, WS_END, 0x00020000);
  const int blk = (lane >> 4) & 1, q4 = (lane & 15) >> 2, p4 = lane & 3;
  const int key0 = (q4 << 2) | (2 * hh), rowb = (8 * hh + q4) * 256;
  STAMP(10);
  BARRIER_L();
  STAMP(11);
  {
    const int off0 = (168 * j) & 63;
    f32x16 prev;
#pragma unroll
    for (int c = 0; c < 3; ++c) {
      const int cb = off0 + 56 * c + 32 * blk + 8 * p4;
      unsigned w0 = ldsb + rowb + (((cb >> 4) ^ key0) << 4) + (cb & 8), w1 = ldsb + rowb + 1024 + (((cb >> 4) ^ key0 ^ 1) << 4) + (cb & 8);
      bf16x8 wf[4];
#pragma unroll
      for (int st = 0; st < 3; ++st) wf[st] = cat(trd_a(w0 + st * 4096), trd_a(w1 + st * 4096));
      f32x16 acc = {};
#pragma unroll
      for (int st = 0; st < 28; ++st) {
        if (st + 3 == 16) { w0 += 65536; w1 += 65536; OPAQUE(w0); OPAQUE(w1); }
        if (st + 3 < 28) wf[(st + 3) & 3] = cat(trd_a(w0 + ((st + 3) & 15) * 4096), trd_a(w1 + ((st + 3) & 15) * 4096));
        acc = MFMA(wf[st & 3], xf[st], acc);
        if (c > 0 && st == 1) qkv_out(prev, 3 * j + c - 1, rstd, xs, rs, rb, lane);
        else SBAR();
        if (c == 2 && (st & 3) == 0 && st >= 4 && st <= 24) BARRIER();
      }
      prev = acc;
      if (c == 0) STAMP(14); if (c == 1) STAMP(15);
    }
    STAMP(2);
    qkv_out(prev, 3 * j + 2, rstd, xs, rs, rb, lane);
  }
  unsigned wb2[4][2];
  bf16x8 wf[2][4];
#pragma unroll
  for (int c = 0; c < 4; ++c) {
    const int cb = 64 * c + 32 * blk + 8 * p4;
    wb2[c][0] = ldsb + 65536 + rowb + (((cb >> 4) ^ key0) << 4) + (cb & 8); wb2[c][1] = ldsb + 65536 + rowb + 1024 + (((cb >> 4) ^ key0 ^ 1) << 4) + (cb & 8);
    wf[0][c] = cat(trd_a(wb2[c][0] + 49152), trd_a(wb2[c][1] + 49152));
  }
  f32x16 hc[4] = {};
  STAMP(18);
#pragma unroll
  for (int p = 0; p < 7; ++p) {
    if (p <= 5) BARRIER();
#pragma unroll
    for (int s = 0; s < 4; ++s) {
      const int st = 4 * p + s;
      const int nb = ((7 + ((st + 1) >> 2)) & 3) * 16384 + ((st + 1) & 3) * 4096;
#pragma unroll
      for (int c = 0; c < 4; ++c) {
        if (st == 3) { wb2[c][0] -= 65536; wb2[c][1] -= 65536; OPAQUE(wb2[c][0]); OPAQUE(wb2[c][1]); }
        if (st == 19) { wb2[c][0] += 65536; wb2[c][1] += 65536; OPAQUE(wb2[c][0]); OPAQUE(wb2[c][1]); }
        if (st < 27) wf[(st + 1) & 1][c] = cat(trd_a(wb2[c][0] + nb), trd_a(wb2[c][1] + nb));
        hc[c] = MFMA(wf[st & 1][c], xf[st], hc[c]);
        SBAR();
      }
    }
    if (p == 3) STAMP(19);
    if (p == PUBP) { publish(lds, C_QKV, 4, ws, WS_FQ + (rt * 16 + j) * 128, tag); STAMP(3); SBAR(); }
  }
  lds_arrive(lds, C_GDONE);
  STAMP(4);
  const int fb0 = 7 * (j >> 1) + (j & 1) * 4;
#pragma unroll
  for (int c = 0; c < 4; ++c) {
    if (c == 3 && (j & 1)) break;
    f32x16 a = hc[c];
#pragma unroll
    for (int e = 0; e < 16; ++e) a[e] = fmaxf(a[e] * rstd, 0.f);
    const unsigned o = WS_H + (rb * 56 + fb0 + c) * 2048 + lane * 16;
    ST_WT(pack8(a, 0), rs, o); ST_WT(pack8(a, 1), rs, o + 1024);
  }
  publish(lds, C_H, 4, ws, WS_FH + (rt * 16 + j) * 128, tag);
  STAMP(5);
}

DI void a_phase1(unsigned ldsb, const char* wqkv, const char* w1, int j, int a, int lane) {
  const int r4 = lane >> 4, cp = lane & 15;
  unsigned vq[4], vf[4];
#pragma unroll
  for (int ii = 0; ii < 4; ++ii) {
    const int row = 16 * a + 4 * ii + r4, ch = cp ^ ((r4 << 2) | ii);
    vq[ii] = row * WQP + ch * 16; vf[ii] = row * W1P + ch * 16;
  }
  const int gran = (cp >> 2) ^ r4;
  const bool actq = gran < 3 || ((168 * j) & 63) > 24;
  const bool actf = gran < 3 || !(j & 1);
  const char* bq = wqkv + ((168 * j) & ~63);
  const char* bf = w1 + (7 * (j >> 1) + (j & 1) * 4) * 64;
  const unsigned dst = ldsb + L_RING + a * 4096;
#define FILL(f) do { _Pragma("unroll") for (int ii = 0; ii < 4; ++ii) { \
    if ((f) < 7) { if (actq) glds(bq + (f) * 64 * WQP, vq[ii], dst + ((f) & 7) * 16384 + ii * 1024); } \
    else { if (actf) glds(bf + ((f) - 7) * 64 * W1P, vf[ii], dst + ((f) & 7) * 16384 + ii * 1024); } } } while (0)
  __builtin_amdgcn_s_sleep(XDELAY);
  FILL(0); FILL(1); FILL(2); FILL(3); FILL(4); FILL(5); FILL(6); FILL(7);
  WAITV(0); BARRIER_L();
  BARRIER(); FILL(8);
  BARRIER(); FILL(9);
  BARRIER(); FILL(10);
  BARRIER(); FILL(11);
  BARRIER(); FILL(12);
  BARRIER(); FILL(13);
  WAITV(20); BARRIER();
  WAITV(16); BARRIER();
  WAITV(12); BARRIER();
  WAITV(8); BARRIER();
  WAITV(4); BARRIER();
  WAITV(0); BARRIER();
#undef FILL
}

#define EX(X, i) X[i] = __builtin_amdgcn_exp2f(X[i])
#define UNIT(CUR, NXT, KN, QN, VP, OP, VC, OC, DO_A, DO_P) do { \
    if (DO_A) NXT = MFMA(KN[0], QN[0], zero); \
    EX(CUR, 0); EX(CUR, 1); EX(CUR, 2); EX(CUR, 3); PIN(CUR); SBAR(); \
    if (DO_P) OP = MFMA(VP[1], __builtin_bit_cast(bf16x8, phi), OP); \
    EX(CUR, 4); EX(CUR, 5); plo[0] = pk2(CUR[0], CUR[1]); EX(CUR, 6); EX(CUR, 7); plo[1] = pk2(CUR[2], CUR[3]); PIN(CUR); PIN(plo); SBAR(); \
    if (DO_A) NXT = MFMA(KN[1], QN[1], NXT); \
    EX(CUR, 8); plo[2] = pk2(CUR[4], CUR[5]); EX(CUR, 9); plo[3] = pk2(CUR[6], CUR[7]); EX(CUR, 10); EX(CUR, 11); PIN(CUR); PIN(plo); SBAR(); \
    OC = MFMA(VC[0], __builtin_bit_cast(bf16x8, plo), OC); \
    EX(CUR, 12); phi[0] = pk2(CUR[8], CUR[9]); EX(CUR, 13); phi[1] = pk2(CUR[10], CUR[11]); EX(CUR, 14); EX(CUR, 15); \
    phi[2] = pk2(CUR[12], CUR[13]); phi[3] = pk2(CUR[14], CUR[15]); PIN(phi); SBAR(); \
  } while (0)

DI void a_attn(LAS char* lds, char* ws, int head, int qt, int a, int lane, u64 tag PROF_ARG) {
  const int hh = lane >> 5;
  const __amdgpu_buffer_rsrc_t rs = __builtin_amdgcn_make_buffer_rsrc(ws, 0, WS_END, 0x00020000);
  {
    const int jq = head / 3, jk = (16 + head) / 3, jv = (32 + head) / 3;
    const int rtl = lane < 8 ? 4 * a + (lane & 3) : qt, jl = lane < 4 ? jk : lane < 8 ? jv : jq;
    poll_flags(ws, WS_FQ + (rtl * 16 + jl) * 128, tag);
  }
  STAMP(2);
  const unsigned qp = WS_QKV + (head * 64 + qt * 4) * 2048, vo = lane * 16;
  const unsigned kp = WS_QKV + ((16 + head) * 64 + a * 16) * 2048, vp = WS_QKV + ((32 + head) * 64 + a * 16) * 2048;
#define LDF(p, t, s) LD_DS(rs, vo + (s) * 1024, (p) + (t) * 2048)
  bf16x8 q0[2], q1[2], q2[2], q3[2], k0[2], k1[2], k2[2], k3[2], v0[2], v1[2], v2[2], v3[2];
  k0[0] = LDF(kp, 0, 0); k0[1] = LDF(kp, 0, 1); q0[0] = LDF(qp, 0, 0); q0[1] = LDF(qp, 0, 1); SBAR();
  q1[0] = LDF(qp, 1, 0); q1[1] = LDF(qp, 1, 1); SBAR();
  q2[0] = LDF(qp, 2, 0); q2[1] = LDF(qp, 2, 1); SBAR(); q3[0] = LDF(qp, 3, 0); q3[1] = LDF(qp, 3, 1); SBAR();
  v0[0] = LDF(vp, 0, 0); v0[1] = LDF(vp, 0, 1);
  k1[0] = LDF(kp, 1, 0); k1[1] = LDF(kp, 1, 1); v1[0] = LDF(vp, 1, 0); v1[1] = LDF(vp, 1, 1);
  k2[0] = LDF(kp, 2, 0); k2[1] = LDF(kp, 2, 1); v2[0] = LDF(vp, 2, 0); v2[1] = LDF(vp, 2, 1);
  k3[0] = LDF(kp, 3, 0); k3[1] = LDF(kp, 3, 1); v3[0] = LDF(vp, 3, 0); v3[1] = LDF(vp, 3, 1);
  SBAR();
  __builtin_amdgcn_s_setprio(2);
  const f32x16 zero = {};
  float mref[4];
#define SETREF(Q, i) do { f32x16 s_ = MFMA(k0[0], Q[0], zero); s_ = MFMA(k0[1], Q[1], s_); \
    float m_ = fmaxf(fmaxf(s_[0], s_[1]), s_[2]); \
    _Pragma("unroll") for (int e = 3; e < 15; e += 2) m_ = fmaxf(fmaxf(m_, s_[e]), s_[e + 1]); \
    m_ = xhalf_max(fmaxf(m_, s_[15])) + SHIFT; \
    const float h_ = rbf(m_), l_ = rbf(m_ - h_); mref[i] = h_ + l_; \
    u32x4 t_ = __builtin_bit_cast(u32x4, Q[1]); t_[2] = hh ? pk2(-h_, -l_) : t_[2]; Q[1] = __builtin_bit_cast(bf16x8, t_); } while (0)
  SETREF(q0, 0); SETREF(q1, 1); SETREF(q2, 2); SETREF(q3, 3);
  f32x16 o0 = {}, o1 = {}, o2 = {}, o3 = {}, sa, sb;
  u32x4 plo, phi = {};
  sa = MFMA(k0[0], q0[0], zero); sa = MFMA(k0[1], q0[1], sa);
  SBAR();
  STAMP(3);
#define TILE(KC, VC, VP, KN, T, PF, LAST) do { \
    UNIT(sa, sb, KC, q1, VP, o3, VC, o0, true, true); \
    if (PF) { VP[0] = LDF(vp, (T) + 3, 0); VP[1] = LDF(vp, (T) + 3, 1); SBAR(); } \
    UNIT(sb, sa, KC, q2, VC, o0, VC, o1, true, true); \
    UNIT(sa, sb, KC, q3, VC, o1, VC, o2, true, true); \
    if (PF && (T) + 4 < 16) { KC[0] = LDF(kp, (T) + 4, 0); KC[1] = LDF(kp, (T) + 4, 1); SBAR(); } \
    UNIT(sb, sa, KN, q0, VC, o2, VC, o3, !(LAST), true); \
  } while (0)
  {
    UNIT(sa, sb, k0, q1, v0, o3, v0, o0, true, false);
    UNIT(sb, sa, k0, q2, v0, o0, v0, o1, true, true);
    UNIT(sa, sb, k0, q3, v0, o1, v0, o2, true, true);
    k0[0] = LDF(kp, 4, 0); k0[1] = LDF(kp, 4, 1); SBAR();
    UNIT(sb, sa, k1, q0, v0, o2, v0, o3, true, true);
  }
#pragma unroll 1
  for (int t = 1; t < 13; t += 4) {
    const unsigned kp_ = kp, vp_ = vp;
    { const unsigned kp = kp_ + t * 2048, vp = vp_ + t * 2048;
      TILE(k1, v1, v0, k2, 0, true, false);
      TILE(k2, v2, v1, k3, 1, true, false);
      TILE(k3, v3, v2, k0, 2, true, false);
      UNIT(sa, sb, k0, q1, v3, o3, v0, o0, true, true);
      v3[0] = LDF(vp, 6, 0); v3[1] = LDF(vp, 6, 1); SBAR();
      UNIT(sb, sa, k0, q2, v0, o0, v0, o1, true, true);
      UNIT(sa, sb, k0, q3, v0, o1, v0, o2, true, true);
      if (t < 9) { k0[0] = LDF(kp, 7, 0); k0[1] = LDF(kp, 7, 1); } SBAR();
      UNIT(sb, sa, k1, q0, v0, o2, v0, o3, true, true);
    }
  }
  TILE(k1, v1, v0, k2, 13, false, false);
  TILE(k2, v2, v1, k3, 14, false, false);
  TILE(k3, v3, v2, k0, 15, false, true);
  o3 = MFMA(v3[1], __builtin_bit_cast(bf16x8, phi), o3);
  __builtin_amdgcn_s_setprio(0);
  STAMP(4);
  lds_wait(lds, C_GDONE, 4);
  LAS float* mst = (LAS float*)(lds + L_MST);
#define PUTO(O, qb) do { const float l_ = hi_half(O[12]); \
    if (lane < 32) { mst[((a * 4 + qb) * 2 + 0) * 32 + lane] = mref[qb]; mst[((a * 4 + qb) * 2 + 1) * 32 + lane] = l_; } \
    if (qb != a) { LAS char* d_ = lds + L_MRG + (a * 3 + (qb > a ? qb - 1 : qb)) * 4096 + lane * 16; \
      _Pragma("unroll") for (int g = 0; g < 4; ++g) *(LAS f32x4*)(d_ + g * 1024) = (f32x4){O[4 * g], O[4 * g + 1], O[4 * g + 2], O[4 * g + 3]}; } } while (0)
  PUTO(o0, 0); PUTO(o1, 1); PUTO(o2, 2); PUTO(o3, 3);
  lds_arrive(lds, C_MRG);
  lds_wait(lds, C_MRG, 4);
  f32x16 o = a == 0 ? o0 : a == 1 ? o1 : a == 2 ? o2 : o3;
  bf16x8 qa[2]; qa[0] = a == 0 ? q0[0] : a == 1 ? q1[0] : a == 2 ? q2[0] : q3[0]; qa[1] = a == 0 ? q0[1] : a == 1 ? q1[1] : a == 2 ? q2[1] : q3[1];
  float mw[4], lw[4]; bool bad = false; float M = -INFINITY;
#pragma unroll
  for (int s = 0; s < 4; ++s) {
    mw[s] = mst[((s * 4 + a) * 2 + 0) * 32 + (lane & 31)]; lw[s] = mst[((s * 4 + a) * 2 + 1) * 32 + (lane & 31)];
    bad |= !(lw[s] > 0x1p-70f && lw[s] < 0x1p90f);
    M = fmaxf(M, mw[s] + __builtin_amdgcn_logf(lw[s]));
  }
  {
    const float ma = a == 0 ? mw[0] : a == 1 ? mw[1] : a == 2 ? mw[2] : mw[3];
    const float f = __builtin_amdgcn_exp2f(ma - M);
#pragma unroll
    for (int e = 0; e < 16; ++e) o[e] *= f;
  }
#pragma unroll
  for (int s = 0; s < 4; ++s) {
    if (s == a) continue;
    const float f = __builtin_amdgcn_exp2f(mw[s] - M);
    LAS const char* sp = lds + L_MRG + (s * 3 + (a > s ? a - 1 : a)) * 4096 + lane * 16;
#pragma unroll
    for (int g = 0; g < 4; ++g) { const f32x4 t = *(LAS const f32x4*)(sp + g * 1024);
#pragma unroll
      for (int e = 0; e < 4; ++e) o[4 * g + e] = fmaf(t[e], f, o[4 * g + e]); }
  }
  {
    float chk = 0.f;
#pragma unroll
    for (int e = 0; e < 16; ++e) chk = fmaf(fabsf(o[e]), 0x1p-8f, chk);
    bad |= !(chk < 0x1p100f);
  }
  if (__any(bad)) {
    u32x4 t_ = __builtin_bit_cast(u32x4, qa[1]); t_[2] = hh ? 0u : t_[2]; qa[1] = __builtin_bit_cast(bf16x8, t_);
    const unsigned kq = kp - a * 16 * 2048, vq = vp - a * 16 * 2048;
    float m = -INFINITY; o = zero;
#pragma unroll 1
    for (int t = 0; t < 64; ++t) {
      f32x16 s_ = MFMA(LDF(kq, t, 0), qa[0], zero); s_ = MFMA(LDF(kq, t, 1), qa[1], s_);
      float mx = s_[0];
#pragma unroll
      for (int e = 1; e < 16; ++e) mx = fmaxf(mx, s_[e]);
      const float mn = fmaxf(m, xhalf_max(mx));
      const float al = __builtin_amdgcn_exp2f(m - mn);
      m = mn;
#pragma unroll
      for (int e = 0; e < 16; ++e) { o[e] *= al; s_[e] = __builtin_amdgcn_exp2f(s_[e] - mn); }
      o = MFMA(LDF(vq, t, 0), __builtin_bit_cast(bf16x8, pack8(s_, 0)), o);
      o = MFMA(LDF(vq, t, 1), __builtin_bit_cast(bf16x8, pack8(s_, 1)), o);
    }
  }
  {
    const float rl = 1.0f / hi_half(o[12]);
#pragma unroll
    for (int e = 0; e < 16; ++e) o[e] *= rl;
    if (hh) { o[12] = 0.f; o[13] = 0.f; o[14] = 0.f; o[15] = 0.f; }
    const unsigned off = WS_O + ((qt * 4 + a) * 16 + head) * 2048 + lane * 16;
    ST_WT(pack8(o, 0), rs, off); ST_WT(pack8(o, 1), rs, off + 1024);
  }
  STAMP(5);
  publish(lds, C_O, 4, ws, WS_FO + (qt * 16 + head) * 128, tag);
  STAMP(6);
}

DI void g_phase2(LAS char* lds, unsigned ldsb, const char* x, const char* wo, const char* w2, char* ws, char* out, int rt64, int ct, int g, int lane, u64 tag PROF_ARG) {
  const int hh = lane >> 5, blk = (lane >> 4) & 1, q4 = (lane & 15) >> 2, p4 = lane & 3;
  const unsigned ring = L_G2 + g * 24576;
  const unsigned va = lane * 16;
  const unsigned vb0 = (lane >> 3) * XP + (((lane & 7) ^ (((lane >> 4) & 1) << 2)) * 16), vb1 = vb0 + 8 * XP;
  const char* hb0 = ws + WS_H + (size_t)((2 * rt64) * 56 + 14 * g) * 2048;
  const char* hb1 = hb0 + 56 * 2048;
  const char* wb = w2 + (size_t)(14 * g * 32) * XP + ct * 128;
  unsigned tb[2][2];
#pragma unroll
  for (int c = 0; c < 2; ++c)
#pragma unroll
    for (int t = 0; t < 2; ++t) tb[c][t] = (8 * t + 4 * hh + q4) * 128 + ((c ^ (q4 >> 1)) * 64) + blk * 32 + p4 * 8;
  bf16x8 ar[6][2];
#define LDA(dst, base) asm volatile("global_load_dwordx4 %0, %1, %2 sc1" : "=v"(dst) : "v"(va), "s"(base) : "memory")
#define ISSUE_B(st) do { glds(wb + (st) * 16 * XP, vb0, ldsb + ring + ((st) % 12) * 2048); glds(wb + (st) * 16 * XP, vb1, ldsb + ring + ((st) % 12) * 2048 + 1024); } while (0)
#define ISSUE_A(st) do { LDA(ar[(st) % 6][0], hb0 + (st) * 1024); LDA(ar[(st) % 6][1], hb1 + (st) * 1024); } while (0)
  const unsigned vw3 = (min(16 + 8 + (lane >> 3), DH - 1)) * XP + (((lane & 7) ^ (((lane >> 4) & 1) << 2)) * 16);
#define ISSUE_W(i) do { const char* b_ = wo + (size_t)((4 * g + (i)) * DH) * XP + ct * 128; const unsigned d_ = ldsb + ring + ((28 + 2 * (i)) % 12) * 2048; \
    glds(b_, vb0, d_); glds(b_, vb1, d_ + 1024); glds(b_ + 16 * XP, vb0, d_ + 2048); glds(b_, vw3, d_ + 3072); } while (0)
  lds_wait(lds, C_GDONE, 4);
  ISSUE_B(0); ISSUE_B(1); ISSUE_B(2); ISSUE_B(3); ISSUE_B(4);
  poll_flags(ws, WS_FH + ((rt64 >> 1) * 16 + 4 * g + (lane & 3)) * 128, tag);
  STAMP(6);
  ISSUE_A(0); ISSUE_A(1); ISSUE_A(2); ISSUE_A(3); ISSUE_A(4);
  f32x16 acc[2][2] = {};
  LAS const char* rp = lds + ring;
#define STEP(st, N) do { \
    asm volatile("s_waitcnt vmcnt(" #N ")" : "+v"(ar[(st) % 6][0]), "+v"(ar[(st) % 6][1]) :: "memory"); \
    LAS const char* b_ = rp + ((st) % 12) * 2048; \
    const bf16x8 w0 = cat(trd(b_ + tb[0][0]), trd(b_ + tb[0][1])), w1 = cat(trd(b_ + tb[1][0]), trd(b_ + tb[1][1])); \
    if ((st) + 5 < 28) { ISSUE_B((st) + 5); ISSUE_A((st) + 5); } else if ((st) + 5 < 32) { ISSUE_W((st) + 5 - 28); } \
    acc[0][0] = MFMA(w0, ar[(st) % 6][0], acc[0][0]); acc[0][1] = MFMA(w1, ar[(st) % 6][0], acc[0][1]); \
    acc[1][0] = MFMA(w0, ar[(st) % 6][1], acc[1][0]); acc[1][1] = MFMA(w1, ar[(st) % 6][1], acc[1][1]); \
    SBAR(); } while (0)
  STEP(0, 8); STEP(1, 10); STEP(2, 12); STEP(3, 14); STEP(4, 16); STEP(5, 16); STEP(6, 16); STEP(7, 16); STEP(8, 16); STEP(9, 16);
  STEP(10, 16); STEP(11, 16); STEP(12, 16); STEP(13, 16); STEP(14, 16); STEP(15, 16); STEP(16, 16); STEP(17, 16); STEP(18, 16); STEP(19, 16);
  STEP(20, 16); STEP(21, 16); STEP(22, 16); STEP(23, 16); STEP(24, 16); STEP(25, 16); STEP(26, 16); STEP(27, 16);
  WAITV(0);
  bf16x8 wf[8][2];
#pragma unroll
  for (int k = 0; k < 8; ++k)
#pragma unroll
    for (int c = 0; c < 2; ++c) { LAS const char* b_ = rp + ((28 + k) % 12) * 2048; wf[k][c] = cat(trd(b_ + tb[c][0]), trd(b_ + tb[c][1])); }
  const int tok0 = rt64 * 64 + g * 16 + (lane >> 3);
  const size_t xo = (size_t)tok0 * XP + ct * 128 + (lane & 7) * 16;
  const u32x4 xres0 = *(const u32x4*)(x + xo), xres1 = *(const u32x4*)(x + xo + 8 * XP);
  STAMP(7);
  poll_flags(ws, WS_FO + ((rt64 >> 1) * 16 + 4 * g + (lane & 3)) * 128, tag);
  STAMP(8);
  {
    const __amdgpu_buffer_rsrc_t rs = __builtin_amdgcn_make_buffer_rsrc(ws, 0, WS_END, 0x00020000);
    const unsigned ob = WS_O + ((2 * rt64) * 16 + 4 * g) * 2048;
    bf16x8 of[2][8];
#pragma unroll
    for (int k = 0; k < 8; ++k) { of[0][k] = LD_DS(rs, va, ob + k * 1024); of[1][k] = LD_DS(rs, va, ob + 16 * 2048 + k * 1024); }
    SBAR();
#pragma unroll
    for (int k = 0; k < 8; ++k)
#pragma unroll
      for (int a = 0; a < 2; ++a)
#pragma unroll
        for (int c = 0; c < 2; ++c) acc[a][c] = MFMA(wf[k][c], of[a][k], acc[a][c]);
  }
  {
    LAS char* d = lds + ring + (lane & 31) * 272 + hh * 16;
#pragma unroll
    for (int a = 0; a < 2; ++a)
#pragma unroll
      for (int c = 0; c < 2; ++c)
#pragma unroll
        for (int gq = 0; gq < 4; ++gq)
          *(LAS f32x4*)(d + a * 32 * 272 + c * 128 + gq * 32) = (f32x4){acc[a][c][4 * gq], acc[a][c][4 * gq + 1], acc[a][c][4 * gq + 2], acc[a][c][4 * gq + 3]};
  }
  lds_arrive(lds, C_RED);
  lds_wait(lds, C_RED, 4);
#pragma unroll
  for (int it = 0; it < 2; ++it) {
    const u32x4 xr = it ? xres1 : xres0;
    float v[8];
#pragma unroll
    for (int e = 0; e < 4; ++e) { v[2 * e] = __uint_as_float(xr[e] << 16); v[2 * e + 1] = __uint_as_float(xr[e] & 0xffff0000u); }
    LAS const char* sp = lds + L_G2 + (g * 16 + it * 8 + (lane >> 3)) * 272 + (lane & 7) * 32;
#pragma unroll
    for (int s = 0; s < 4; ++s) {
      const f32x4 t0 = *(LAS const f32x4*)(sp + s * 24576), t1 = *(LAS const f32x4*)(sp + s * 24576 + 16);
#pragma unroll
      for (int e = 0; e < 4; ++e) { v[e] += t0[e]; v[4 + e] += t1[e]; }
    }
    *(u32x4*)(out + xo + it * 8 * XP) = (u32x4){pk2(v[0], v[1]), pk2(v[2], v[3]), pk2(v[4], v[5]), pk2(v[6], v[7])};
  }
  STAMP(9);
}

__global__ void __launch_bounds__(512, 2) fused_block(const char* __restrict__ x, const char* __restrict__ wqkv, const char* __restrict__ wo,
                                                      const char* __restrict__ w1, const char* __restrict__ w2, char* out, char* ws) {
  extern __shared__ __attribute__((aligned(16))) char smem[];
  LAS char* lds = (LAS char*)smem;
  const unsigned ldsb = (unsigned)(uintptr_t)lds;
  const int lane = threadIdx.x & 63, wave = __builtin_amdgcn_readfirstlane(threadIdx.x >> 6);
  const int xcd = blockIdx.x & 7, slot = blockIdx.x >> 3;
  STAMP(0);
  const u64 epoch = __hip_atomic_load((GAS u64*)(ws + WS_EPOCH), RLX_AGENT);
  if (threadIdx.x < 16) ((LAS unsigned*)(lds + L_CNT))[threadIdx.x] = 0u;
  const int rt = 8 * (xcd & 1) + (slot & 7), j = 4 * (xcd >> 1) + (slot >> 3);
  if (wave < 4) {
    g_phase1(lds, ldsb, x, ws, rt, j, wave, lane, epoch PROF_PASS);
    const u64 tag = mk_tag(epoch);
    int lane2 = lane; OPAQUE(lane2);
    if (slot < 28) g_phase2(lds, ldsb, x, wo, w2, ws, out, 4 * xcd + slot / 7, slot % 7, wave, lane2, tag PROF_PASS);
    else if (blockIdx.x == 255 && wave == 0) {
#pragma unroll
      for (int i = 0; i < 4; ++i) poll_flags(ws, WS_FQ + (i * 64 + lane) * 128, tag);
      if (lane == 0) __hip_atomic_store((GAS u64*)(ws + WS_EPOCH), tag, RLX_AGENT);
    }
  } else {
    a_phase1(ldsb, wqkv, w1, j, wave - 4, lane);
    STAMP(1);
    a_attn(lds, ws, 2 * xcd + (slot >> 4), slot & 15, wave - 4, lane, mk_tag(epoch) PROF_PASS);
  }
}

extern "C" void kernel_launch(void* const* d_in, const int* in_sizes, int n_in, void* d_out, int out_size, void* d_ws, size_t ws_size, hipStream_t stream) {
  (void)in_sizes; (void)n_in; (void)out_size; (void)ws_size;
  (void)hipFuncSetAttribute((const void*)fused_block, hipFuncAttributeMaxDynamicSharedMemorySize, LDS_BYTES);
  fused_block<<<256, 512, LDS_BYTES, stream>>>((const char*)d_in[0], (const char*)d_in[1], (const char*)d_in[2], (const char*)d_in[4], (const char*)d_in[5], (char*)d_out, (char*)d_ws);
}
```

```cpp
#define PF_TLB 1
#include <hip/hip_runtime.h>
#include <hip/hip_bf16.h>
#include <stdint.h>

typedef short bf16x8 __attribute__((ext_vector_type(8)));
typedef short s16x4 __attribute__((ext_vector_type(4)));
typedef float f32x16 __attribute__((ext_vector_type(16)));
typedef float f32x4 __attribute__((ext_vector_type(4)));
typedef unsigned u32x4 __attribute__((ext_vector_type(4)));
typedef __bf16 bf16x2 __attribute__((ext_vector_type(2)));
typedef unsigned long long u64;
#define GAS __attribute__((address_space(1)))
#define LAS __attribute__((address_space(3)))
#define DI __device__ __forceinline__
#define MFMA(a, b, c) __builtin_amdgcn_mfma_f32_32x32x16_bf16((a), (b), (c), 0, 0, 0)
#define SBAR() __builtin_amdgcn_sched_barrier(0)
#define PIN(x) asm volatile("" : "+v"(x))
#define RLX_AGENT __ATOMIC_RELAXED, __HIP_MEMORY_SCOPE_AGENT
#define WAITV(n) asm volatile("s_waitcnt vmcnt(" #n ")" ::: "memory")
#define BARRIER() asm volatile("s_barrier" ::: "memory")
#define BARRIER_L() asm volatile("s_waitcnt lgkmcnt(0)\n\ts_barrier" ::: "memory")

constexpr int S = 2048, D = 448, H = 16, DH = 28, F = 1792;
constexpr int XP = D * 2;
constexpr int WQP = 3 * D * 2;
constexpr int W1P = F * 2;
constexpr float QSCALE = 0.18898223650461362f * 1.4426950408889634f;
constexpr float SHIFT = 60.f;

constexpr int WS_EPOCH = 0;
constexpr int WS_FQ = 4096;
constexpr int WS_FH = WS_FQ + 256 * 128;
constexpr int WS_FO = WS_FH + 256 * 128;
constexpr int WS_PROF = 128 * 1024;
constexpr int WS_QKV = 1 << 20;
constexpr int WS_H = WS_QKV + 48 * 64 * 2048;
constexpr int WS_O = WS_H + 64 * 56 * 2048;
constexpr int WS_END = WS_O + 64 * 16 * 2048;

constexpr int L_RING = 0;
constexpr int L_XS = 131072;
constexpr int L_G2 = 0;
constexpr int L_MRG = 98304;
constexpr int L_MST = 147456;
constexpr int L_CNT = L_MST + 4096;
constexpr int LDS_BYTES = 163840;
constexpr int L_RSTD = L_CNT + 64;
#define PUBP 0
#define XDELAY 16
constexpr unsigned SPIN_CAP = 1u << 18;

#define PROF_ARG
#define PROF_PASS
#define STAMP(i) do {} while (0)

DI unsigned pk2(float lo, float hi) { bf16x2 r = {(__bf16)lo, (__bf16)hi}; return __builtin_bit_cast(unsigned, r); }
DI float rbf(float v) { return (float)(__bf16)v; }
DI float dot2(unsigned a, unsigned b, float c) { return __builtin_amdgcn_fdot2_f32_bf16(__builtin_bit_cast(bf16x2, a), __builtin_bit_cast(bf16x2, b), c, false); }
template <int CTRL> DI float dpp(float x) { return __builtin_bit_cast(float, __builtin_amdgcn_mov_dpp(__builtin_bit_cast(int, x), CTRL, 0xf, 0xf, true)); }
DI float sum8(float v) { v += dpp<0xB1>(v); v += dpp<0x4E>(v); v += dpp<0x141>(v); return v; }
DI float xhalf_max(float m) { auto r = __builtin_amdgcn_permlane32_swap(__float_as_uint(m), __float_as_uint(m), false, false); return fmaxf(__uint_as_float(r[0]), __uint_as_float(r[1])); }
DI float hi_half(float m) { auto r = __builtin_amdgcn_permlane32_swap(__float_as_uint(m), __float_as_uint(m), false, false); return __uint_as_float(r[1]); }
DI s16x4 trd(LAS const char* p) { typedef short v4s __attribute__((ext_vector_type(4))); return __builtin_bit_cast(s16x4, __builtin_amdgcn_ds_read_tr16_b64_v4i16((LAS v4s*)p)); }
DI bf16x8 cat(s16x4 a, s16x4 b) { return __builtin_shufflevector(a, b, 0, 1, 2, 3, 4, 5, 6, 7); }
DI void glds(const void* sbase, unsigned voff, unsigned lds_dst) {
  unsigned keep;
  asm volatile("s_mov_b32 %0, m0\n\ts_mov_b32 m0, %3\n\ts_nop 0\n\tglobal_load_lds_dwordx4 %1, %2\n\ts_mov_b32 m0, %0"
               : "=&s"(keep) : "v"(voff), "s"(sbase), "s"(lds_dst) : "memory");
}
DI unsigned lds_arrive(LAS char* lds, int idx) {
  unsigned r = 0;
  asm volatile("s_waitcnt lgkmcnt(0)" ::: "memory");
  if ((threadIdx.x & 63) == 0) r = __hip_atomic_fetch_add((LAS unsigned*)(lds + L_CNT) + idx, 1u, __ATOMIC_RELAXED, __HIP_MEMORY_SCOPE_WORKGROUP);
  return __builtin_amdgcn_readfirstlane(r);
}
DI void lds_wait(LAS char* lds, int idx, unsigned n) {
#pragma unroll 1
  for (unsigned sp = 0; sp < SPIN_CAP; ++sp) {
    unsigned v = __builtin_amdgcn_readfirstlane(__hip_atomic_load((LAS unsigned*)(lds + L_CNT) + idx, __ATOMIC_RELAXED, __HIP_MEMORY_SCOPE_WORKGROUP));
    if (v >= n) break;
    __builtin_amdgcn_s_sleep(1);
  }
  asm volatile("" ::: "memory");
}
DI void poll_flags(char* ws, unsigned off, u64 tag) {
  GAS u64* p = (GAS u64*)(ws + off);
#pragma unroll 1
  for (unsigned sp = 0; sp < SPIN_CAP; ++sp) {
    u64 v = __hip_atomic_load(p, RLX_AGENT);
    if (__all(v == tag)) break;
    __builtin_amdgcn_s_sleep(2);
  }
  __builtin_amdgcn_fence(__ATOMIC_ACQUIRE, "wavefront");
  asm volatile("" ::: "memory");
}
DI void publish(LAS char* lds, int cnt, unsigned n, char* ws, unsigned off, u64 tag) {
  WAITV(0);
  unsigned old = lds_arrive(lds, cnt);
  if (old == n - 1 && (threadIdx.x & 63) == 0) __hip_atomic_store((GAS u64*)(ws + off), tag, RLX_AGENT);
}
DI u32x4 pack8(const f32x16& a, int s) {
  return (u32x4){pk2(a[8 * s], a[8 * s + 1]), pk2(a[8 * s + 2], a[8 * s + 3]), pk2(a[8 * s + 4], a[8 * s + 5]), pk2(a[8 * s + 6], a[8 * s + 7])};
}
#define LD_DS(rsrc, voff, soff) __builtin_bit_cast(bf16x8, __builtin_amdgcn_raw_buffer_load_b128((rsrc), (voff), (soff), 16))
#define ST_WT(v, rsrc, off) __builtin_amdgcn_raw_buffer_store_b128((v), (rsrc), (off), 0, 16)

enum { C_QKV = 0, C_H = 1, C_GDONE = 2, C_MRG = 3, C_O = 4, C_RED = 5 };

DI u32x4 center8(u32x4 w, float negm) {
  float t0, t1, t2, t3, t4, t5, t6, t7; u32x4 r;
  asm("v_dot2_f32_bf16 %4, %12, %17, %16\n\tv_dot2_f32_bf16 %5, %12, %18, %16\n\t"
      "v_dot2_f32_bf16 %6, %13, %17, %16\n\tv_dot2_f32_bf16 %7, %13, %18, %16\n\t"
      "v_dot2_f32_bf16 %8, %14, %17, %16\n\tv_dot2_f32_bf16 %9, %14, %18, %16\n\t"
      "v_dot2_f32_bf16 %10, %15, %17, %16\n\tv_dot2_f32_bf16 %11, %15, %18, %16\n\t"
      "v_cvt_pk_bf16_f32 %0, %4, %5\n\tv_cvt_pk_bf16_f32 %1, %6, %7\n\tv_cvt_pk_bf16_f32 %2, %8, %9\n\tv_cvt_pk_bf16_f32 %3, %10, %11"
      : "=&v"(r[0]), "=&v"(r[1]), "=&v"(r[2]), "=&v"(r[3]), "=&v"(t0), "=&v"(t1), "=&v"(t2), "=&v"(t3), "=&v"(t4), "=&v"(t5), "=&v"(t6), "=&v"(t7)
      : "v"(w[0]), "v"(w[1]), "v"(w[2]), "v"(w[3]), "v"(negm), "s"(0x00003f80u), "s"(0x3f800000u));
  return r;
}
DI u64 mk_tag(u64 e) { e += 1; return ((u64)(unsigned)__builtin_amdgcn_readfirstlane((unsigned)(e >> 32)) << 32) | (unsigned)__builtin_amdgcn_readfirstlane((unsigned)e); }
DI s16x4 trd_a(unsigned a) { return trd((LAS const char*)(uintptr_t)a); }
#define OPAQUE(x) asm volatile("" : "+v"(x))

DI void qkv_out(f32x16 a, int b, float rstd, LAS char* xs, __amdgpu_buffer_rsrc_t rs, int rb, int lane) {
  const int r = lane & 31, hh = lane >> 5;
  const float sc = b < 16 ? rstd * QSCALE : rstd;
#pragma unroll
  for (int e = 0; e < 16; ++e) a[e] *= sc;
  if (hh) { a[12] = b < 16 ? 0.f : 1.f; a[13] = (b >= 16 && b < 32) ? 1.f : 0.f; a[14] = 0.f; a[15] = 0.f; }
  u32x4 f0 = pack8(a, 0), f1 = pack8(a, 1);
  if (b >= 32) {
    typedef unsigned u32x2 __attribute__((ext_vector_type(2)));
    LAS char* im = xs + 2048;
    LAS char* wp = im + r * 64 + hh * 8;
    *(LAS u32x2*)(wp) = (u32x2){f0[0], f0[1]}; *(LAS u32x2*)(wp + 16) = (u32x2){f0[2], f0[3]};
    *(LAS u32x2*)(wp + 32) = (u32x2){f1[0], f1[1]}; *(LAS u32x2*)(wp + 48) = (u32x2){f1[2], f1[3]};
    LAS const char* rp = im + (4 * hh + ((lane & 15) >> 2)) * 64 + ((lane >> 4) & 1) * 32 + (lane & 3) * 8;
    f0 = __builtin_bit_cast(u32x4, cat(trd(rp), trd(rp + 512)));
    f1 = __builtin_bit_cast(u32x4, cat(trd(rp + 1024), trd(rp + 1536)));
  }
  const unsigned o = WS_QKV + (b * 64 + rb) * 2048 + lane * 16;
  ST_WT(f0, rs, o); ST_WT(f1, rs, o + 1024);
}

DI void g_phase1(LAS char* lds, unsigned ldsb, const char* x, char* ws, int rt, int j, int w, int lane, u64 epoch PROF_ARG) {
  const int row0 = rt * 128 + w * 32, rb = rt * 4 + w;
  const int r = lane & 31, hh = lane >> 5;
  LAS char* xs = lds + L_XS + w * 4096;
  bf16x8 xf[28];
  float rstd;
  {
    u32x4 xr[7][4];
    const char* xb = x + (size_t)(row0 + (lane >> 3)) * XP + (lane & 7) * 16;
#pragma unroll
    for (int p = 0; p < 7; ++p)
#pragma unroll
      for (int i = 0; i < 4; ++i) xr[p][i] = *(const u32x4*)(xb + i * 8 * XP + p * 128);
    SBAR();
    float sm[4] = {}, sq[4] = {};
#pragma unroll
    for (int p = 0; p < 7; ++p) {
#pragma unroll
      for (int d = 0; d < 4; ++d)
#pragma unroll
        for (int i = 0; i < 4; ++i) { sm[i] = dot2(xr[p][i][d], 0x3f803f80u, sm[i]); sq[i] = dot2(xr[p][i][d], xr[p][i][d], sq[i]); }
      SBAR();
    }
    STAMP(1);
    float negm[4];
    LAS float* rsl = (LAS float*)(lds + L_RSTD + w * 128);
#pragma unroll
    for (int i = 0; i < 4; ++i) {
      const float mean = sum8(sm[i]) * (1.0f / D);
      negm[i] = -rbf(mean);
      float v = sum8(sq[i]) * (1.0f / D) - mean * mean;
      v = rbf(fmaxf(v, 0.f));
      const float sd = rbf(__builtin_sqrtf(rbf(v + 1e-5f)));
      if ((lane & 7) == 0) rsl[i * 8 + (lane >> 3)] = __builtin_amdgcn_rcpf(sd);
    }
    STAMP(16);
    LAS char* xs_w0 = xs + (lane >> 3) * 128 + (((lane & 7) ^ ((lane >> 4) & 3)) * 16);
    LAS char* xs_w1 = xs + (lane >> 3) * 128 + (((lane & 7) ^ ((lane >> 4) & 3) ^ 4) * 16);
#pragma unroll
    for (int p = 0; p < 7; ++p) {
#pragma unroll
      for (int i = 0; i < 4; ++i) *(LAS u32x4*)(((i & 1) ? xs_w1 : xs_w0) + i * 1024) = center8(xr[p][i], negm[i]);
#pragma unroll
      for (int s = 0; s < 4; ++s) xf[4 * p + s] = *(LAS const bf16x8*)(xs + r * 128 + (((2 * s + hh) ^ ((r >> 1) & 7)) * 16));
      if (p == 2) STAMP(17);
    }
    rstd = rsl[r];
  }
  const u64 tag = mk_tag(epoch);
  const __amdgpu_buffer_rsrc_t rs = __builtin_amdgcn_make_buffer_rsrc(ws, 0, WS_END, 0x00020000);
  const int blk = (lane >> 4) & 1, q4 = (lane & 15) >> 2, p4 = lane & 3;
  const int key0 = (q4 << 2) | (2 * hh), rowb = (8 * hh + q4) * 256;
  STAMP(10);
  BARRIER_L();
  STAMP(11);
  {
    const int off0 = (168 * j) & 63;
    f32x16 prev;
#pragma unroll
    for (int c = 0; c < 3; ++c) {
      const int cb = off0 + 56 * c + 32 * blk + 8 * p4;
      unsigned w0 = ldsb + rowb + (((cb >> 4) ^ key0) << 4) + (cb & 8), w1 = ldsb + rowb + 1024 + (((cb >> 4) ^ key0 ^ 1) << 4) + (cb & 8);
      bf16x8 wf[4];
#pragma unroll
      for (int st = 0; st < 3; ++st) wf[st] = cat(trd_a(w0 + st * 4096), trd_a(w1 + st * 4096));
      f32x16 acc = {};
#pragma unroll
      for (int st = 0; st < 28; ++st) {
        if (st + 3 == 16) { w0 += 65536; w1 += 65536; OPAQUE(w0); OPAQUE(w1); }
        if (st + 3 < 28) wf[(st + 3) & 3] = cat(trd_a(w0 + ((st + 3) & 15) * 4096), trd_a(w1 + ((st + 3) & 15) * 4096));
        acc = MFMA(wf[st & 3], xf[st], acc);
        if (c > 0 && st == 1) qkv_out(prev, 3 * j + c - 1, rstd, xs, rs, rb, lane);
        else SBAR();
        if (c == 2 && (st & 3) == 0 && st >= 4 && st <= 24) BARRIER();
      }
      prev = acc;
      if (c == 0) STAMP(14); if (c == 1) STAMP(15);
    }
    STAMP(2);
    qkv_out(prev, 3 * j + 2, rstd, xs, rs, rb, lane);
  }
  unsigned wb2[4][2];
  bf16x8 wf[2][4];
#pragma unroll
  for (int c = 0; c < 4; ++c) {
    const int cb = 64 * c + 32 * blk + 8 * p4;
    wb2[c][0] = ldsb + 65536 + rowb + (((cb >> 4) ^ key0) << 4) + (cb & 8); wb2[c][1] = ldsb + 65536 + rowb + 1024 + (((cb >> 4) ^ key0 ^ 1) << 4) + (cb & 8);
    wf[0][c] = cat(trd_a(wb2[c][0] + 49152), trd_a(wb2[c][1] + 49152));
  }
  f32x16 hc[4] = {};
  STAMP(18);
#pragma unroll
  for (int p = 0; p < 7; ++p) {
    if (p <= 5) BARRIER();
#pragma unroll
    for (int s = 0; s < 4; ++s) {
      const int st = 4 * p + s;
      const int nb = ((7 + ((st + 1) >> 2)) & 3) * 16384 + ((st + 1) & 3) * 4096;
#pragma unroll
      for (int c = 0; c < 4; ++c) {
        if (st == 3) { wb2[c][0] -= 65536; wb2[c][1] -= 65536; OPAQUE(wb2[c][0]); OPAQUE(wb2[c][1]); }
        if (st == 19) { wb2[c][0] += 65536; wb2[c][1] += 65536; OPAQUE(wb2[c][0]); OPAQUE(wb2[c][1]); }
        if (st < 27) wf[(st + 1) & 1][c] = cat(trd_a(wb2[c][0] + nb), trd_a(wb2[c][1] + nb));
        hc[c] = MFMA(wf[st & 1][c], xf[st], hc[c]);
        SBAR();
      }
    }
    if (p == 3) STAMP(19);
    if (p == PUBP) { publish(lds, C_QKV, 4, ws, WS_FQ + (rt * 16 + j) * 128, tag); STAMP(3); SBAR(); }
  }
  lds_arrive(lds, C_GDONE);
  STAMP(4);
  const int fb0 = 7 * (j >> 1) + (j & 1) * 4;
#pragma unroll
  for (int c = 0; c < 4; ++c) {
    if (c == 3 && (j & 1)) break;
    f32x16 a = hc[c];
#pragma unroll
    for (int e = 0; e < 16; ++e) a[e] = fmaxf(a[e] * rstd, 0.f);
    const unsigned o = WS_H + (rb * 56 + fb0 + c) * 2048 + lane * 16;
    ST_WT(pack8(a, 0), rs, o); ST_WT(pack8(a, 1), rs, o + 1024);
  }
  publish(lds, C_H, 4, ws, WS_FH + (rt * 16 + j) * 128, tag);
  STAMP(5);
}

DI void a_phase1(unsigned ldsb, const char* wqkv, const char* w1, int j, int a, int lane) {
  const int r4 = lane >> 4, cp = lane & 15;
  unsigned vq[4], vf[4];
#pragma unroll
  for (int ii = 0; ii < 4; ++ii) {
    const int row = 16 * a + 4 * ii + r4, ch = cp ^ ((r4 << 2) | ii);
    vq[ii] = row * WQP + ch * 16; vf[ii] = row * W1P + ch * 16;
  }
  const int gran = (cp >> 2) ^ r4;
  const bool actq = gran < 3 || ((168 * j) & 63) > 24;
  const bool actf = gran < 3 || !(j & 1);
  const char* bq = wqkv + ((168 * j) & ~63);
  const char* bf = w1 + (7 * (j >> 1) + (j & 1) * 4) * 64;
  const unsigned dst = ldsb + L_RING + a * 4096;
#define FILL(f) do { _Pragma("unroll") for (int ii = 0; ii < 4; ++ii) { \
    if ((f) < 7) { if (actq) glds(bq + (f) * 64 * WQP, vq[ii], dst + ((f) & 7) * 16384 + ii * 1024); } \
    else { if (actf) glds(bf + ((f) - 7) * 64 * W1P, vf[ii], dst + ((f) & 7) * 16384 + ii * 1024); } } } while (0)
  __builtin_amdgcn_s_sleep(XDELAY);
  FILL(0); FILL(1); FILL(2); FILL(3); FILL(4); FILL(5); FILL(6); FILL(7);
  WAITV(0); BARRIER_L();
  BARRIER(); FILL(8);
  BARRIER(); FILL(9);
  BARRIER(); FILL(10);
  BARRIER(); FILL(11);
  BARRIER(); FILL(12);
  BARRIER(); FILL(13);
  WAITV(20); BARRIER();
  WAITV(16); BARRIER();
  WAITV(12); BARRIER();
  WAITV(8); BARRIER();
  WAITV(4); BARRIER();
  WAITV(0); BARRIER();
#undef FILL
}

#define EX(X, i) X[i] = __builtin_amdgcn_exp2f(X[i])
#define UNIT(CUR, NXT, KN, QN, VP, OP, VC, OC, DO_A, DO_P) do { \
    if (DO_A) NXT = MFMA(KN[0], QN[0], zero); \
    EX(CUR, 0); EX(CUR, 1); EX(CUR, 2); EX(CUR, 3); PIN(CUR); SBAR(); \
    if (DO_P) OP = MFMA(VP[1], __builtin_bit_cast(bf16x8, phi), OP); \
    EX(CUR, 4); EX(CUR, 5); plo[0] = pk2(CUR[0], CUR[1]); EX(CUR, 6); EX(CUR, 7); plo[1] = pk2(CUR[2], CUR[3]); PIN(CUR); PIN(plo); SBAR(); \
    if (DO_A) NXT = MFMA(KN[1], QN[1], NXT); \
    EX(CUR, 8); plo[2] = pk2(CUR[4], CUR[5]); EX(CUR, 9); plo[3] = pk2(CUR[6], CUR[7]); EX(CUR, 10); EX(CUR, 11); PIN(CUR); PIN(plo); SBAR(); \
    OC = MFMA(VC[0], __builtin_bit_cast(bf16x8, plo), OC); \
    EX(CUR, 12); phi[0] = pk2(CUR[8], CUR[9]); EX(CUR, 13); phi[1] = pk2(CUR[10], CUR[11]); EX(CUR, 14); EX(CUR, 15); \
    phi[2] = pk2(CUR[12], CUR[13]); phi[3] = pk2(CUR[14], CUR[15]); PIN(phi); SBAR(); \
  } while (0)

DI void a_attn(LAS char* lds, char* ws, int head, int qt, int a, int lane, u64 tag PROF_ARG) {
  const int hh = lane >> 5;
  const __amdgpu_buffer_rsrc_t rs = __builtin_amdgcn_make_buffer_rsrc(ws, 0, WS_END, 0x00020000);
  {
    const int jq = head / 3, jk = (16 + head) / 3, jv = (32 + head) / 3;
    const int rtl = lane < 8 ? 4 * a + (lane & 3) : qt, jl = lane < 4 ? jk : lane < 8 ? jv : jq;
    poll_flags(ws, WS_FQ + (rtl * 16 + jl) * 128, tag);
  }
  STAMP(2);
  const unsigned qp = WS_QKV + (head * 64 + qt * 4) * 2048, vo = lane * 16;
  const unsigned kp = WS_QKV + ((16 + head) * 64 + a * 16) * 2048, vp = WS_QKV + ((32 + head) * 64 + a * 16) * 2048;
#define LDF(p, t, s) LD_DS(rs, vo + (s) * 1024, (p) + (t) * 2048)
  bf16x8 q0[2], q1[2], q2[2], q3[2], k0[2], k1[2], k2[2], k3[2], v0[2], v1[2], v2[2], v3[2];
  k0[0] = LDF(kp, 0, 0); k0[1] = LDF(kp, 0, 1); q0[0] = LDF(qp, 0, 0); q0[1] = LDF(qp, 0, 1); SBAR();
  q1[0] = LDF(qp, 1, 0); q1[1] = LDF(qp, 1, 1); SBAR();
  q2[0] = LDF(qp, 2, 0); q2[1] = LDF(qp, 2, 1); SBAR(); q3[0] = LDF(qp, 3, 0); q3[1] = LDF(qp, 3, 1); SBAR();
  v0[0] = LDF(vp, 0, 0); v0[1] = LDF(vp, 0, 1);
  k1[0] = LDF(kp, 1, 0); k1[1] = LDF(kp, 1, 1); v1[0] = LDF(vp, 1, 0); v1[1] = LDF(vp, 1, 1);
  k2[0] = LDF(kp, 2, 0); k2[1] = LDF(kp, 2, 1); v2[0] = LDF(vp, 2, 0); v2[1] = LDF(vp, 2, 1);
  k3[0] = LDF(kp, 3, 0); k3[1] = LDF(kp, 3, 1); v3[0] = LDF(vp, 3, 0); v3[1] = LDF(vp, 3, 1);
  SBAR();
  __builtin_amdgcn_s_setprio(2);
  const f32x16 zero = {};
  float mref[4];
#define SETREF(Q, i) do { f32x16 s_ = MFMA(k0[0], Q[0], zero); s_ = MFMA(k0[1], Q[1], s_); \
    float m_ = fmaxf(fmaxf(s_[0], s_[1]), s_[2]); \
    _Pragma("unroll") for (int e = 3; e < 15; e += 2) m_ = fmaxf(fmaxf(m_, s_[e]), s_[e + 1]); \
    m_ = xhalf_max(fmaxf(m_, s_[15])) + SHIFT; \
    const float h_ = rbf(m_), l_ = rbf(m_ - h_); mref[i] = h_ + l_; \
    u32x4 t_ = __builtin_bit_cast(u32x4, Q[1]); t_[2] = hh ? pk2(-h_, -l_) : t_[2]; Q[1] = __builtin_bit_cast(bf16x8, t_); } while (0)
  SETREF(q0, 0); SETREF(q1, 1); SETREF(q2, 2); SETREF(q3, 3);
  f32x16 o0 = {}, o1 = {}, o2 = {}, o3 = {}, sa, sb;
  u32x4 plo, phi = {};
  sa = MFMA(k0[0], q0[0], zero); sa = MFMA(k0[1], q0[1], sa);
  SBAR();
  STAMP(3);
#define TILE(KC, VC, VP, KN, T, PF, LAST) do { \
    UNIT(sa, sb, KC, q1, VP, o3, VC, o0, true, true); \
    if (PF) { VP[0] = LDF(vp, (T) + 3, 0); VP[1] = LDF(vp, (T) + 3, 1); SBAR(); } \
    UNIT(sb, sa, KC, q2, VC, o0, VC, o1, true, true); \
    UNIT(sa, sb, KC, q3, VC, o1, VC, o2, true, true); \
    if (PF && (T) + 4 < 16) { KC[0] = LDF(kp, (T) + 4, 0); KC[1] = LDF(kp, (T) + 4, 1); SBAR(); } \
    UNIT(sb, sa, KN, q0, VC, o2, VC, o3, !(LAST), true); \
  } while (0)
  {
    UNIT(sa, sb, k0, q1, v0, o3, v0, o0, true, false);
    UNIT(sb, sa, k0, q2, v0, o0, v0, o1, true, true);
    UNIT(sa, sb, k0, q3, v0, o1, v0, o2, true, true);
    k0[0] = LDF(kp, 4, 0); k0[1] = LDF(kp, 4, 1); SBAR();
    UNIT(sb, sa, k1, q0, v0, o2, v0, o3, true, true);
  }
#pragma unroll 1
  for (int t = 1; t < 13; t += 4) {
    const unsigned kp_ = kp, vp_ = vp;
    { const unsigned kp = kp_ + t * 2048, vp = vp_ + t * 2048;
      TILE(k1, v1, v0, k2, 0, true, false);
      TILE(k2, v2, v1, k3, 1, true, false);
      TILE(k3, v3, v2, k0, 2, true, false);
      UNIT(sa, sb, k0, q1, v3, o3, v0, o0, true, true);
      v3[0] = LDF(vp, 6, 0); v3[1] = LDF(vp, 6, 1); SBAR();
      UNIT(sb, sa, k0, q2, v0, o0, v0, o1, true, true);
      UNIT(sa, sb, k0, q3, v0, o1, v0, o2, true, true);
      if (t < 9) { k0[0] = LDF(kp, 7, 0); k0[1] = LDF(kp, 7, 1); } SBAR();
      UNIT(sb, sa, k1, q0, v0, o2, v0, o3, true, true);
    }
  }
  TILE(k1, v1, v0, k2, 13, false, false);
  TILE(k2, v2, v1, k3, 14, false, false);
  TILE(k3, v3, v2, k0, 15, false, true);
  o3 = MFMA(v3[1], __builtin_bit_cast(bf16x8, phi), o3);
  __builtin_amdgcn_s_setprio(0);
  STAMP(4);
  lds_wait(lds, C_GDONE, 4);
  LAS float* mst = (LAS float*)(lds + L_MST);
#define PUTO(O, qb) do { const float l_ = hi_half(O[12]); \
    if (lane < 32) { mst[((a * 4 + qb) * 2 + 0) * 32 + lane] = mref[qb]; mst[((a * 4 + qb) * 2 + 1) * 32 + lane] = l_; } \
    if (qb != a) { LAS char* d_ = lds + L_MRG + (a * 3 + (qb > a ? qb - 1 : qb)) * 4096 + lane * 16; \
      _Pragma("unroll") for (int g = 0; g < 4; ++g) *(LAS f32x4*)(d_ + g * 1024) = (f32x4){O[4 * g], O[4 * g + 1], O[4 * g + 2], O[4 * g + 3]}; } } while (0)
  PUTO(o0, 0); PUTO(o1, 1); PUTO(o2, 2); PUTO(o3, 3);
  lds_arrive(lds, C_MRG);
  lds_wait(lds, C_MRG, 4);
  f32x16 o = a == 0 ? o0 : a == 1 ? o1 : a == 2 ? o2 : o3;
  bf16x8 qa[2]; qa[0] = a == 0 ? q0[0] : a == 1 ? q1[0] : a == 2 ? q2[0] : q3[0]; qa[1] = a == 0 ? q0[1] : a == 1 ? q1[1] : a == 2 ? q2[1] : q3[1];
  float mw[4], lw[4]; bool bad = false; float M = -INFINITY;
#pragma unroll
  for (int s = 0; s < 4; ++s) {
    mw[s] = mst[((s * 4 + a) * 2 + 0) * 32 + (lane & 31)]; lw[s] = mst[((s * 4 + a) * 2 + 1) * 32 + (lane & 31)];
    bad |= !(lw[s] > 0x1p-70f && lw[s] < 0x1p90f);
    M = fmaxf(M, mw[s] + __builtin_amdgcn_logf(lw[s]));
  }
  {
    const float ma = a == 0 ? mw[0] : a == 1 ? mw[1] : a == 2 ? mw[2] : mw[3];
    const float f = __builtin_amdgcn_exp2f(ma - M);
#pragma unroll
    for (int e = 0; e < 16; ++e) o[e] *= f;
  }
#pragma unroll
  for (int s = 0; s < 4; ++s) {
    if (s == a) continue;
    const float f = __builtin_amdgcn_exp2f(mw[s] - M);
    LAS const char* sp = lds + L_MRG + (s * 3 + (a > s ? a - 1 : a)) * 4096 + lane * 16;
#pragma unroll
    for (int g = 0; g < 4; ++g) { const f32x4 t = *(LAS const f32x4*)(sp + g * 1024);
#pragma unroll
      for (int e = 0; e < 4; ++e) o[4 * g + e] = fmaf(t[e], f, o[4 * g + e]); }
  }
  {
    float chk = 0.f;
#pragma unroll
    for (int e = 0; e < 16; ++e) chk = fmaf(fabsf(o[e]), 0x1p-8f, chk);
    bad |= !(chk < 0x1p100f);
  }
  if (__any(bad)) {
    u32x4 t_ = __builtin_bit_cast(u32x4, qa[1]); t_[2] = hh ? 0u : t_[2]; qa[1] = __builtin_bit_cast(bf16x8, t_);
    const unsigned kq = kp - a * 16 * 2048, vq = vp - a * 16 * 2048;
    float m = -INFINITY; o = zero;
#pragma unroll 1
    for (int t = 0; t < 64; ++t) {
      f32x16 s_ = MFMA(LDF(kq, t, 0), qa[0], zero); s_ = MFMA(LDF(kq, t, 1), qa[1], s_);
      float mx = s_[0];
#pragma unroll
      for (int e = 1; e < 16; ++e) mx = fmaxf(mx, s_[e]);
      const float mn = fmaxf(m, xhalf_max(mx));
      const float al = __builtin_amdgcn_exp2f(m - mn);
      m = mn;
#pragma unroll
      for (int e = 0; e < 16; ++e) { o[e] *= al; s_[e] = __builtin_amdgcn_exp2f(s_[e] - mn); }
      o = MFMA(LDF(vq, t, 0), __builtin_bit_cast(bf16x8, pack8(s_, 0)), o);
      o = MFMA(LDF(vq, t, 1), __builtin_bit_cast(bf16x8, pack8(s_, 1)), o);
    }
  }
  {
    const float rl = 1.0f / hi_half(o[12]);
#pragma unroll
    for (int e = 0; e < 16; ++e) o[e] *= rl;
    if (hh) { o[12] = 0.f; o[13] = 0.f; o[14] = 0.f; o[15] = 0.f; }
    const unsigned off = WS_O + ((qt * 4 + a) * 16 + head) * 2048 + lane * 16;
    ST_WT(pack8(o, 0), rs, off); ST_WT(pack8(o, 1), rs, off + 1024);
  }
  STAMP(5);
  publish(lds, C_O, 4, ws, WS_FO + (qt * 16 + head) * 128, tag);
  STAMP(6);
}

DI void g_phase2(LAS char* lds, unsigned ldsb, const char* x, const char* wo, const char* w2, char* ws, char* out, int rt64, int ct, int g, int lane, u64 tag PROF_ARG) {
  const int hh = lane >> 5, blk = (lane >> 4) & 1, q4 = (lane & 15) >> 2, p4 = lane & 3;
  const unsigned ring = L_G2 + g * 24576;
  const unsigned va = lane * 16;
  const unsigned vb0 = (lane >> 3) * XP + (((lane & 7) ^ (((lane >> 4) & 1) << 2)) * 16), vb1 = vb0 + 8 * XP;
  const char* hb0 = ws + WS_H + (size_t)((2 * rt64) * 56 + 14 * g) * 2048;
  const char* hb1 = hb0 + 56 * 2048;
  const char* wb = w2 + (size_t)(14 * g * 32) * XP + ct * 128;
  unsigned tb[2][2];
#pragma unroll
  for (int c = 0; c < 2; ++c)
#pragma unroll
    for (int t = 0; t < 2; ++t) tb[c][t] = (8 * t + 4 * hh + q4) * 128 + ((c ^ (q4 >> 1)) * 64) + blk * 32 + p4 * 8;
  bf16x8 ar[6][2];
#define LDA(dst, base) asm volatile("global_load_dwordx4 %0, %1, %2 sc1" : "=v"(dst) : "v"(va), "s"(base) : "memory")
#define ISSUE_B(st) do { glds(wb + (st) * 16 * XP, vb0, ldsb + ring + ((st) % 12) * 2048); glds(wb + (st) * 16 * XP, vb1, ldsb + ring + ((st) % 12) * 2048 + 1024); } while (0)
#define ISSUE_A(st) do { LDA(ar[(st) % 6][0], hb0 + (st) * 1024); LDA(ar[(st) % 6][1], hb1 + (st) * 1024); } while (0)
  const unsigned vw3 = (min(16 + 8 + (lane >> 3), DH - 1)) * XP + (((lane & 7) ^ (((lane >> 4) & 1) << 2)) * 16);
#define ISSUE_W(i) do { const char* b_ = wo + (size_t)((4 * g + (i)) * DH) * XP + ct * 128; const unsigned d_ = ldsb + ring + ((28 + 2 * (i)) % 12) * 2048; \
    glds(b_, vb0, d_); glds(b_, vb1, d_ + 1024); glds(b_ + 16 * XP, vb0, d_ + 2048); glds(b_, vw3, d_ + 3072); } while (0)
  lds_wait(lds, C_GDONE, 4);
  ISSUE_B(0); ISSUE_B(1); ISSUE_B(2); ISSUE_B(3); ISSUE_B(4);
  poll_flags(ws, WS_FH + ((rt64 >> 1) * 16 + 4 * g + (lane & 3)) * 128, tag);
  STAMP(6);
  ISSUE_A(0); ISSUE_A(1); ISSUE_A(2); ISSUE_A(3); ISSUE_A(4);
  f32x16 acc[2][2] = {};
  LAS const char* rp = lds + ring;
#define STEP(st, N) do { \
    asm volatile("s_waitcnt vmcnt(" #N ")" : "+v"(ar[(st) % 6][0]), "+v"(ar[(st) % 6][1]) :: "memory"); \
    LAS const char* b_ = rp + ((st) % 12) * 2048; \
    const bf16x8 w0 = cat(trd(b_ + tb[0][0]), trd(b_ + tb[0][1])), w1 = cat(trd(b_ + tb[1][0]), trd(b_ + tb[1][1])); \
    if ((st) + 5 < 28) { ISSUE_B((st) + 5); ISSUE_A((st) + 5); } else if ((st) + 5 < 32) { ISSUE_W((st) + 5 - 28); } \
    acc[0][0] = MFMA(w0, ar[(st) % 6][0], acc[0][0]); acc[0][1] = MFMA(w1, ar[(st) % 6][0], acc[0][1]); \
    acc[1][0] = MFMA(w0, ar[(st) % 6][1], acc[1][0]); acc[1][1] = MFMA(w1, ar[(st) % 6][1], acc[1][1]); \
    SBAR(); } while (0)
  STEP(0, 8); STEP(1, 10); STEP(2, 12); STEP(3, 14); STEP(4, 16); STEP(5, 16); STEP(6, 16); STEP(7, 16); STEP(8, 16); STEP(9, 16);
  STEP(10, 16); STEP(11, 16); STEP(12, 16); STEP(13, 16); STEP(14, 16); STEP(15, 16); STEP(16, 16); STEP(17, 16); STEP(18, 16); STEP(19, 16);
  STEP(20, 16); STEP(21, 16); STEP(22, 16); STEP(23, 16); STEP(24, 16); STEP(25, 16); STEP(26, 16); STEP(27, 16);
  WAITV(0);
  bf16x8 wf[8][2];
#pragma unroll
  for (int k = 0; k < 8; ++k)
#pragma unroll
    for (int c = 0; c < 2; ++c) { LAS const char* b_ = rp + ((28 + k) % 12) * 2048; wf[k][c] = cat(trd(b_ + tb[c][0]), trd(b_ + tb[c][1])); }
  const int tok0 = rt64 * 64 + g * 16 + (lane >> 3);
  const size_t xo = (size_t)tok0 * XP + ct * 128 + (lane & 7) * 16;
  const u32x4 xres0 = *(const u32x4*)(x + xo), xres1 = *(const u32x4*)(x + xo + 8 * XP);
  STAMP(7);
  poll_flags(ws, WS_FO + ((rt64 >> 1) * 16 + 4 * g + (lane & 3)) * 128, tag);
  STAMP(8);
  {
    const __amdgpu_buffer_rsrc_t rs = __builtin_amdgcn_make_buffer_rsrc(ws, 0, WS_END, 0x00020000);
    const unsigned ob = WS_O + ((2 * rt64) * 16 + 4 * g) * 2048;
    bf16x8 of[2][8];
#pragma unroll
    for (int k = 0; k < 8; ++k) { of[0][k] = LD_DS(rs, va, ob + k * 1024); of[1][k] = LD_DS(rs, va, ob + 16 * 2048 + k * 1024); }
    SBAR();
#pragma unroll
    for (int k = 0; k < 8; ++k)
#pragma unroll
      for (int a = 0; a < 2; ++a)
#pragma unroll
        for (int c = 0; c < 2; ++c) acc[a][c] = MFMA(wf[k][c], of[a][k], acc[a][c]);
  }
  {
    LAS char* d = lds + ring + (lane & 31) * 272 + hh * 16;
#pragma unroll
    for (int a = 0; a < 2; ++a)
#pragma unroll
      for (int c = 0; c < 2; ++c)
#pragma unroll
        for (int gq = 0; gq < 4; ++gq)
          *(LAS f32x4*)(d + a * 32 * 272 + c * 128 + gq * 32) = (f32x4){acc[a][c][4 * gq], acc[a][c][4 * gq + 1], acc[a][c][4 * gq + 2], acc[a][c][4 * gq + 3]};
  }
  lds_arrive(lds, C_RED);
  lds_wait(lds, C_RED, 4);
#pragma unroll
  for (int it = 0; it < 2; ++it) {
    const u32x4 xr = it ? xres1 : xres0;
    float v[8];
#pragma unroll
    for (int e = 0; e < 4; ++e) { v[2 * e] = __uint_as_float(xr[e] << 16); v[2 * e + 1] = __uint_as_float(xr[e] & 0xffff0000u); }
    LAS const char* sp = lds + L_G2 + (g * 16 + it * 8 + (lane >> 3)) * 272 + (lane & 7) * 32;
#pragma unroll
    for (int s = 0; s < 4; ++s) {
      const f32x4 t0 = *(LAS const f32x4*)(sp + s * 24576), t1 = *(LAS const f32x4*)(sp + s * 24576 + 16);
#pragma unroll
      for (int e = 0; e < 4; ++e) { v[e] += t0[e]; v[4 + e] += t1[e]; }
    }
    *(u32x4*)(out + xo + it * 8 * XP) = (u32x4){pk2(v[0], v[1]), pk2(v[2], v[3]), pk2(v[4], v[5]), pk2(v[6], v[7])};
  }
  STAMP(9);
}

__global__ void __launch_bounds__(512, 2) fused_block(const char* __restrict__ x, const char* __restrict__ wqkv, const char* __restrict__ wo,
                                                      const char* __restrict__ w1, const char* __restrict__ w2, char* out, char* ws) {
  u64 pc0; asm volatile("s_getpc_b64 %0" : "=s"(pc0));
  extern __shared__ __attribute__((aligned(16))) char smem[];
  LAS char* lds = (LAS char*)smem;
  const unsigned ldsb = (unsigned)(uintptr_t)lds;
  const int lane = threadIdx.x & 63, wave = __builtin_amdgcn_readfirstlane(threadIdx.x >> 6);
  const int xcd = blockIdx.x & 7, slot = blockIdx.x >> 3;
  STAMP(0);
  const u64 epoch = __hip_atomic_load((GAS u64*)(ws + WS_EPOCH), RLX_AGENT);
  if (threadIdx.x < 16) ((LAS unsigned*)(lds + L_CNT))[threadIdx.x] = 0u;
  const int rt = 8 * (xcd & 1) + (slot & 7), j = 4 * (xcd >> 1) + (slot >> 3);
  if (wave < 4) {
    g_phase1(lds, ldsb, x, ws, rt, j, wave, lane, epoch PROF_PASS);
    const u64 tag = mk_tag(epoch);
    int lane2 = lane; OPAQUE(lane2);
    if (slot < 28) g_phase2(lds, ldsb, x, wo, w2, ws, out, 4 * xcd + slot / 7, slot % 7, wave, lane2, tag PROF_PASS);
    else if (blockIdx.x == 255 && wave == 0) {
#pragma unroll
      for (int i = 0; i < 4; ++i) poll_flags(ws, WS_FQ + (i * 64 + lane) * 128, tag);
      if (lane == 0) __hip_atomic_store((GAS u64*)(ws + WS_EPOCH), tag, RLX_AGENT);
    }
  } else {
    u32x4 pfc = {}; unsigned pft = 0;
    { const char* p = wave == 4 ? ws + lane * (WS_END / 64) : lane < 16 ? out + lane * (S * XP / 16) : lane < 32 ? w2 + (lane - 16) * (F * XP / 16) : lane < 40 ? wo + (lane - 32) * (D * XP / 8) : w1 + (lane - 40) * (D * W1P / 24);
      if (wave < 6) asm volatile("global_load_dword %0, %1, off sc1" : "+v"(pft) : "v"(p) : "memory"); }
    a_phase1(ldsb, wqkv, w1, j, wave - 4, lane);
    asm volatile("" :: "v"(pfc), "v"(pft));
    STAMP(1);
    a_attn(lds, ws, 2 * xcd + (slot >> 4), slot & 15, wave - 4, lane, mk_tag(epoch) PROF_PASS);
  }
}

extern "C" void kernel_launch(void* const* d_in, const int* in_sizes, int n_in, void* d_out, int out_size, void* d_ws, size_t ws_size, hipStream_t stream) {
  (void)in_sizes; (void)n_in; (void)out_size; (void)ws_size;
  (void)hipFuncSetAttribute((const void*)fused_block, hipFuncAttributeMaxDynamicSharedMemorySize, LDS_BYTES);
  fused_block<<<256, 512, LDS_BYTES, stream>>>((const char*)d_in[0], (const char*)d_in[1], (const char*)d_in[2], (const char*)d_in[4], (const char*)d_in[5], (char*)d_out, (char*)d_ws);
}
```

```cpp
#include <hip/hip_runtime.h>
#include <hip/hip_bf16.h>
#include <stdint.h>

typedef short bf16x8 __attribute__((ext_vector_type(8)));
typedef short s16x4 __attribute__((ext_vector_type(4)));
typedef float f32x16 __attribute__((ext_vector_type(16)));
typedef float f32x4 __attribute__((ext_vector_type(4)));
typedef unsigned u32x4 __attribute__((ext_vector_type(4)));
typedef __bf16 bf16x2 __attribute__((ext_vector_type(2)));
typedef unsigned long long u64;
#define GAS __attribute__((address_space(1)))
#define LAS __attribute__((address_space(3)))
#define DI __device__ __forceinline__
#define MFMA(a, b, c) __builtin_amdgcn_mfma_f32_32x32x16_bf16((a), (b), (c), 0, 0, 0)
#define SBAR() __builtin_amdgcn_sched_barrier(0)
#define PIN(x) asm volatile("" : "+v"(x))
#define RLX_AGENT __ATOMIC_RELAXED, __HIP_MEMORY_SCOPE_AGENT
#define WAITV(n) asm volatile("s_waitcnt vmcnt(" #n ")" ::: "memory")
#define WAITL() asm volatile("s_waitcnt lgkmcnt(0)" ::: "memory")
#define BARRIER() asm volatile("s_barrier" ::: "memory")
#define BARRIER_L() asm volatile("s_waitcnt lgkmcnt(0)\n\ts_barrier" ::: "memory")

constexpr int S = 2048, D = 448, H = 16, DH = 28, F = 1792;
constexpr int XP = D * 2;
constexpr int WQP = 3 * D * 2;
constexpr int W1P = F * 2;
constexpr float QSCALE = 0.18898223650461362f * 1.4426950408889634f;
constexpr float SHIFT = 60.f;

constexpr int WS_EPOCH = 0;
constexpr int WS_FQ = 4096;
constexpr int WS_FH = WS_FQ + 256 * 128;
constexpr int WS_FO = WS_FH + 256 * 128;
constexpr int WS_PROF = 128 * 1024;
constexpr int WS_QKV = 1 << 20;
constexpr int WS_H = WS_QKV + 48 * 64 * 2048;
constexpr int WS_O = WS_H + 64 * 56 * 2048;
constexpr int WS_END = WS_O + 64 * 16 * 2048;

constexpr int SLAB = 28672;
constexpr int L_P01 = 114688;
constexpr int L_S0 = 81920, L_S1 = L_P01, L_S2 = 0, L_S3 = 32768;
constexpr int L_VT = 147456;
constexpr int L_G2 = 0;
constexpr int L_MRG = 98304;
constexpr int L_MST = 147456;
constexpr int L_CNT = 159744;
constexpr int LDS_BYTES = 163840;
constexpr unsigned SPIN_CAP = 1u << 18;
#define LSLEEP 1
#define PD 3
#define PUBST 15
#define XDELAY 16

#define PROF_ARG
#define PROF_PASS
#define STAMP(i) do {} while (0)

DI unsigned pk2(float lo, float hi) { bf16x2 r = {(__bf16)lo, (__bf16)hi}; return __builtin_bit_cast(unsigned, r); }
DI float rbf(float v) { return (float)(__bf16)v; }
DI float dot2(unsigned a, unsigned b, float c) { return __builtin_amdgcn_fdot2_f32_bf16(__builtin_bit_cast(bf16x2, a), __builtin_bit_cast(bf16x2, b), c, false); }
template <int CTRL> DI float dpp(float x) { return __builtin_bit_cast(float, __builtin_amdgcn_mov_dpp(__builtin_bit_cast(int, x), CTRL, 0xf, 0xf, true)); }
DI float sum8(float v) { v += dpp<0xB1>(v); v += dpp<0x4E>(v); v += dpp<0x141>(v); return v; }
DI float xhalf_max(float m) { auto r = __builtin_amdgcn_permlane32_swap(__float_as_uint(m), __float_as_uint(m), false, false); return fmaxf(__uint_as_float(r[0]), __uint_as_float(r[1])); }
DI float xhalf_sum(float m) { auto r = __builtin_amdgcn_permlane32_swap(__float_as_uint(m), __float_as_uint(m), false, false); return __uint_as_float(r[0]) + __uint_as_float(r[1]); }
DI float hi_half(float m) { auto r = __builtin_amdgcn_permlane32_swap(__float_as_uint(m), __float_as_uint(m), false, false); return __uint_as_float(r[1]); }
DI s16x4 trd(LAS const char* p) { typedef short v4s __attribute__((ext_vector_type(4))); return __builtin_bit_cast(s16x4, __builtin_amdgcn_ds_read_tr16_b64_v4i16((LAS v4s*)p)); }
DI bf16x8 cat(s16x4 a, s16x4 b) { return __builtin_shufflevector(a, b, 0, 1, 2, 3, 4, 5, 6, 7); }
DI void glds(const void* sbase, unsigned voff, unsigned lds_dst) {
  unsigned keep;
  asm volatile("s_mov_b32 %0, m0\n\ts_mov_b32 m0, %3\n\ts_nop 0\n\tglobal_load_lds_dwordx4 %1, %2\n\ts_mov_b32 m0, %0"
               : "=&s"(keep) : "v"(voff), "s"(sbase), "s"(lds_dst) : "memory");
}
DI void lds_signal(unsigned ldsb, int idx) {
  const unsigned ad = ldsb + L_CNT + idx * 4, one = 1u;
  asm volatile("s_mov_b64 exec, 1\n\tds_add_u32 %0, %1\n\ts_mov_b64 exec, -1" :: "v"(ad), "v"(one) : "memory");
}
DI void lds_wait(LAS char* lds, int idx, unsigned n) {
#pragma unroll 1
  for (unsigned sp = 0; sp < SPIN_CAP; ++sp) {
    unsigned v = __builtin_amdgcn_readfirstlane(__hip_atomic_load((LAS unsigned*)(lds + L_CNT) + idx, __ATOMIC_RELAXED, __HIP_MEMORY_SCOPE_WORKGROUP));
    if (v >= n) break;
    __builtin_amdgcn_s_sleep(LSLEEP);
  }
  asm volatile("" ::: "memory");
}
DI void poll_flags(char* ws, unsigned off, u64 tag) {
  GAS u64* p = (GAS u64*)(ws + off);
#pragma unroll 1
  for (unsigned sp = 0; sp < SPIN_CAP; ++sp) {
    u64 v = __hip_atomic_load(p, RLX_AGENT);
    if (__all(v == tag)) break;
    __builtin_amdgcn_s_sleep(2);
  }
  __builtin_amdgcn_fence(__ATOMIC_ACQUIRE, "wavefront");
  asm volatile("" ::: "memory");
}
DI void publish(char* ws, unsigned off, u64 tag) {
  WAITV(0);
  if ((threadIdx.x & 63) == 0) __hip_atomic_store((GAS u64*)(ws + off), tag, RLX_AGENT);
}
#define CNT_LD(idx) __hip_atomic_load((LAS unsigned*)(lds + L_CNT) + (idx), __ATOMIC_RELAXED, __HIP_MEMORY_SCOPE_WORKGROUP)
#define CNT_CHECK(v, idx, n) do { if (__builtin_expect(__builtin_amdgcn_readfirstlane(v) < (n), 0)) lds_wait(lds, (idx), (n)); asm volatile("" ::: "memory"); } while (0)
DI u32x4 pack8(const f32x16& a, int s) {
  return (u32x4){pk2(a[8 * s], a[8 * s + 1]), pk2(a[8 * s + 2], a[8 * s + 3]), pk2(a[8 * s + 4], a[8 * s + 5]), pk2(a[8 * s + 6], a[8 * s + 7])};
}
#define LD_DS(rsrc, voff, soff) __builtin_bit_cast(bf16x8, __builtin_amdgcn_raw_buffer_load_b128((rsrc), (voff), (soff), 16))
#define ST_WT(v, rsrc, off) __builtin_amdgcn_raw_buffer_store_b128((v), (rsrc), (off), 0, 16)

enum { C_AGO = 0, C_GDONE = 2, C_MRG = 3, C_RED = 5, C_XFREE = 6, C_REL = 8, C_LAND = 12, C_PAN = 16, C_XA = 20 };

DI u32x4 center8(u32x4 w, float negm) {
  float t0, t1, t2, t3, t4, t5, t6, t7; u32x4 r;
  asm("v_dot2_f32_bf16 %4, %12, %17, %16\n\tv_dot2_f32_bf16 %5, %12, %18, %16\n\t"
      "v_dot2_f32_bf16 %6, %13, %17, %16\n\tv_dot2_f32_bf16 %7, %13, %18, %16\n\t"
      "v_dot2_f32_bf16 %8, %14, %17, %16\n\tv_dot2_f32_bf16 %9, %14, %18, %16\n\t"
      "v_dot2_f32_bf16 %10, %15, %17, %16\n\tv_dot2_f32_bf16 %11, %15, %18, %16\n\t"
      "v_cvt_pk_bf16_f32 %0, %4, %5\n\tv_cvt_pk_bf16_f32 %1, %6, %7\n\tv_cvt_pk_bf16_f32 %2, %8, %9\n\tv_cvt_pk_bf16_f32 %3, %10, %11"
      : "=&v"(r[0]), "=&v"(r[1]), "=&v"(r[2]), "=&v"(r[3]), "=&v"(t0), "=&v"(t1), "=&v"(t2), "=&v"(t3), "=&v"(t4), "=&v"(t5), "=&v"(t6), "=&v"(t7)
      : "v"(w[0]), "v"(w[1]), "v"(w[2]), "v"(w[3]), "v"(negm), "s"(0x00003f80u), "s"(0x3f800000u));
  return r;
}
DI unsigned max2(unsigned u, short f) {
  typedef short s16x2 __attribute__((ext_vector_type(2)));
  return __builtin_bit_cast(unsigned, __builtin_elementwise_max(__builtin_bit_cast(s16x2, u), (s16x2){f, f}));
}
#define EPOCH_LOAD(e) asm volatile("s_load_dwordx2 %0, %1, 0x0\n\ts_waitcnt lgkmcnt(0)" : "=s"(e) : "s"(ws + WS_EPOCH) : "memory")
DI s16x4 trd_a(unsigned a) { return trd((LAS const char*)(uintptr_t)a); }
#define OPAQUE(x) asm volatile("" : "+v"(x))

#define XDMA_SETUP() const char* xb = x + (size_t)(rt * 128 + w * 32) * XP; \
    const unsigned v0 = (lane >> 3) * XP + (((lane & 7) ^ ((lane >> 4) & 3)) * 16), v1 = (v0 ^ 64) + 8 * XP
#define XDMA(p) do { const unsigned d_ = ldsb + w * 28672 + (p) * 4096; \
    glds(xb + (p) * 128, v0, d_); glds(xb + (p) * 128, v1, d_ + 1024); glds(xb + (p) * 128 + 16 * XP, v0, d_ + 2048); glds(xb + (p) * 128 + 16 * XP, v1, d_ + 3072); } while (0)

DI void g_phase1(LAS char* lds, unsigned ldsb, const char* x, char* ws, int rt, int j, int w, int lane, u64& epoch PROF_ARG) {
  const int rb = rt * 4 + w;
  const int r = lane & 31, hh = lane >> 5;
  bf16x8 xf[28];
  float rstd;
  {
    XDMA_SETUP();
#pragma unroll
    for (int p = 0; p < 4; ++p) XDMA(p);
    EPOCH_LOAD(epoch);
    LAS const char* xi = lds + w * 28672 + r * 128;
    unsigned sw[4];
#pragma unroll
    for (int s = 0; s < 4; ++s) sw[s] = ((2 * s + hh) ^ ((r >> 1) & 7)) * 16;
    float sm[4] = {}, sq[4] = {};
#define XPANEL(p, N) do { WAITV(N); \
      _Pragma("unroll") for (int s = 0; s < 4; ++s) xf[4 * (p) + s] = *(LAS const bf16x8*)(xi + (p) * 4096 + sw[s]); \
      if ((p) > 0) XSUM((p) - 1); SBAR(); } while (0)
#define XSUM(p) do { _Pragma("unroll") for (int s = 0; s < 4; ++s) { const u32x4 u_ = __builtin_bit_cast(u32x4, xf[4 * (p) + s]); \
      _Pragma("unroll") for (int d = 0; d < 4; ++d) { sm[d] = dot2(u_[d], 0x3f803f80u, sm[d]); sq[d] = dot2(u_[d], u_[d], sq[d]); } } } while (0)
    STAMP(17);
    XPANEL(0, 12); STAMP(20); XPANEL(1, 8); XPANEL(2, 4); XPANEL(3, 0);
    STAMP(21);
    lds_wait(lds, C_XA + w, 1);
    STAMP(12);
    XPANEL(4, 0); XPANEL(5, 0); XPANEL(6, 0);
    WAITL();
    lds_signal(ldsb, C_XFREE);
    XSUM(6);
    STAMP(1);
    const float mean = xhalf_sum((sm[0] + sm[1]) + (sm[2] + sm[3])) * (1.0f / D);
    const float msq = xhalf_sum((sq[0] + sq[1]) + (sq[2] + sq[3])) * (1.0f / D);
    const float negm = -rbf(mean);
    rstd = __builtin_amdgcn_rcpf(rbf(__builtin_amdgcn_sqrtf(rbf(rbf(fmaxf(fmaf(-mean, mean, msq), 0.f)) + 1e-5f))));
    STAMP(16);
#pragma unroll
    for (int s = 0; s < 28; ++s) xf[s] = __builtin_bit_cast(bf16x8, center8(__builtin_bit_cast(u32x4, xf[s]), negm));
  }
  const u64 tag = epoch + 1;
  const __amdgpu_buffer_rsrc_t rs = __builtin_amdgcn_make_buffer_rsrc(ws, 0, WS_END, 0x00020000);
  const int c8 = 32 * ((lane >> 4) & 1) + 8 * (lane & 3), q4 = (lane & 15) >> 2;
  const int key0 = (q4 << 2) | (2 * hh), off0 = (168 * j) & 63;
  const int fb0 = 7 * (j >> 1) + (j & 1) * 4;
  const unsigned vo = lane * 16;
  LAS char* vim_w = lds + L_VT + w * 2304 + r * 72 + hh * 8;
  LAS const char* vim_r = lds + L_VT + w * 2304 + (4 * hh + q4) * 72 + c8;
  u32x4 f0, f1, hp[4][2]; float sc;
#define EPI(X, cp, k) do { \
    const int b_ = 3 * j + (cp); \
    if ((k) == 0) { sc = ((cp) < 3 && b_ < 16) ? rstd * QSCALE : rstd; X[0] *= sc; X[1] *= sc; X[2] *= sc; X[3] *= sc; } \
    if ((k) == 1) { X[4] *= sc; X[5] *= sc; X[6] *= sc; X[7] *= sc; f0[0] = pk2(X[0], X[1]); f0[1] = pk2(X[2], X[3]); } \
    if ((k) == 2) { X[8] *= sc; X[9] *= sc; X[10] *= sc; X[11] *= sc; f0[2] = pk2(X[4], X[5]); f0[3] = pk2(X[6], X[7]); } \
    if ((k) == 3) { X[12] *= sc; X[13] *= sc; X[14] *= sc; X[15] *= sc; f1[0] = pk2(X[8], X[9]); f1[1] = pk2(X[10], X[11]); } \
    if ((k) == 4) { f1[2] = pk2(X[12], X[13]); f1[3] = pk2(X[14], X[15]); \
      if ((cp) < 3) {   \
        f1[2] = hh ? (b_ < 16 ? 0u : b_ < 32 ? 0x3f803f80u : 0x00003f80u) : f1[2]; f1[3] = hh ? 0u : f1[3]; } \
      else { f0[0] = max2(f0[0], 0); f0[1] = max2(f0[1], 0); f0[2] = max2(f0[2], 0); f0[3] = max2(f0[3], 0); } } \
    if ((k) == 5) { \
      if ((cp) >= 3) { f1[0] = max2(f1[0], 0); f1[1] = max2(f1[1], 0); f1[2] = max2(f1[2], 0); f1[3] = max2(f1[3], 0); } \
      else if (b_ >= 32) {     \
        typedef unsigned u32x2 __attribute__((ext_vector_type(2))); \
        *(LAS u32x2*)(vim_w) = (u32x2){f0[0], f0[1]}; *(LAS u32x2*)(vim_w + 16) = (u32x2){f0[2], f0[3]}; \
        *(LAS u32x2*)(vim_w + 32) = (u32x2){f1[0], f1[1]}; *(LAS u32x2*)(vim_w + 48) = (u32x2){f1[2], f1[3]}; \
        f0 = __builtin_bit_cast(u32x4, cat(trd(vim_r), trd(vim_r + 576))); f1 = __builtin_bit_cast(u32x4, cat(trd(vim_r + 1152), trd(vim_r + 1728))); } } \
    if ((k) == 8) { \
      if ((cp) < 3) { const unsigned so_ = WS_QKV + (b_ * 64 + rb) * 2048; \
        __builtin_amdgcn_raw_buffer_store_b128(f0, rs, vo, so_, 16); __builtin_amdgcn_raw_buffer_store_b128(f1, rs, vo + 1024, so_, 16); } \
      else { hp[(cp) < 3 ? 0 : (cp) - 3][0] = f0; hp[(cp) < 3 ? 0 : (cp) - 3][1] = f1; } } \
    if ((k) < 4) PIN(X); \
    if ((k) >= 1 && (k) < 5) { PIN(f0); PIN(f1); } } while (0)
  unsigned qa0, qa1;
  const unsigned fa = ldsb + (8 * hh + q4) * 64 + c8;
#define QADDR(c) do { const int cb_ = off0 + 56 * (c) + c8; qa0 = ldsb + L_P01 + (8 * hh + q4) * 256 + (((cb_ >> 4) ^ key0) << 4) + (cb_ & 8); qa1 = (qa0 + 1024) ^ 16; OPAQUE(qa0); OPAQUE(qa1); } while (0)
#define QOFF(st) ((st) < 8 ? (st) * 4096 : (((st) - 8) & 15) * 4096)
#define FRAG(c, st) ((c) < 3 ? cat(trd_a(qa0 + QOFF(st)), trd_a(qa1 + QOFF(st))) : cat(trd_a(fb + (st) * 1024), trd_a(fb + (st) * 1024 + 256)))
  STAMP(10);
  lds_wait(lds, C_PAN + 0, 4);
  STAMP(11);
  bf16x8 wf[8];
  unsigned fb = fa;
  QADDR(0);
#pragma unroll
  for (int st = 0; st < PD; ++st) wf[st] = FRAG(0, st);
  f32x16 A, B;
  const f32x16 zero = {};
  f0 = (u32x4){}; f1 = (u32x4){};
#define WFI(c, st) ((((c) * 28) + (st)) & 7)
#define SWEEP(c, X, Y, HAS_NEXT) do { \
    unsigned landed_ = 0; \
    _Pragma("unroll") for (int st = 0; st < 28; ++st) { \
      if ((c) == 0 && (st == 0 || st == 8)) landed_ = CNT_LD(C_PAN + 1 + st / 8); \
      if ((c) == 0 && (st == 8 - PD || st == 16 - PD)) CNT_CHECK(landed_, C_PAN + 1 + st / 8, 4);     \
      if ((c) < 3 && st == 8 - PD) { qa0 -= L_P01; qa1 -= L_P01; OPAQUE(qa0); OPAQUE(qa1); } \
      if ((c) < 3 && st == 24 - PD) { qa0 += 65536; qa1 += 65536; OPAQUE(qa0); OPAQUE(qa1); } \
      if (HAS_NEXT && (c) >= 2 && st == 24 - PD) landed_ = CNT_LD(C_LAND + (c) - 2); \
      if (HAS_NEXT && st == 28 - PD) { \
        if ((c) < 2) QADDR((c) + 1); \
        else { CNT_CHECK(landed_, C_LAND + (c) - 2, 4); fb = fa + ((c) == 2 ? L_S0 : (c) == 3 ? L_S1 : (c) == 4 ? L_S2 : L_S3); OPAQUE(fb); } } \
      if (st < 28 - PD) wf[WFI(c, st + PD)] = FRAG(c, st + PD); else if (HAS_NEXT) wf[WFI(c, st + PD)] = FRAG((c) + 1, st + PD - 28); \
      X = MFMA(wf[WFI(c, st)], xf[st], st == 0 ? zero : X); \
      if ((c) > 0 && st >= 1 && st <= 9) EPI(Y, (c) - 1, st - 1); \
      SBAR(); \
      if ((c) == 2 && (st == 7 || st == 15 || st == 23)) lds_signal(ldsb, C_REL + 1 + st / 8);     \
      if ((c) == 3 && st == PUBST) { publish(ws, WS_FQ + (rt * 16 + j) * 128 + w * 8, tag); STAMP(3); } \
    } } while (0)
  SWEEP(0, A, B, true); STAMP(14);
  SWEEP(1, B, A, true); STAMP(15);
  SWEEP(2, A, B, true); STAMP(2);
  SWEEP(3, B, A, true); STAMP(18);
  SWEEP(4, A, B, true);
  if (!(j & 1)) {
    SWEEP(5, B, A, true);
    SWEEP(6, A, B, false);
    lds_signal(ldsb, C_GDONE);
    EPI(A, 6, 0); EPI(A, 6, 1); EPI(A, 6, 2); EPI(A, 6, 3); EPI(A, 6, 4); EPI(A, 6, 5); EPI(A, 6, 8);
  } else {
    SWEEP(5, B, A, false);
    hp[3][0] = f0; hp[3][1] = f1;
    lds_signal(ldsb, C_GDONE);
    EPI(B, 5, 0); EPI(B, 5, 1); EPI(B, 5, 2); EPI(B, 5, 3); EPI(B, 5, 4); EPI(B, 5, 5); EPI(B, 5, 8);
  }
  STAMP(4);
  lds_wait(lds, C_AGO, 4);
  STAMP(19);
#pragma unroll
  for (int c = 0; c < 4; ++c) {
    const unsigned so = (c == 3 && (j & 1)) ? 0x7ff00000u : WS_H + (rb * 56 + fb0 + c) * 2048;
    __builtin_amdgcn_raw_buffer_store_b128(hp[c][0], rs, vo, so, 16); __builtin_amdgcn_raw_buffer_store_b128(hp[c][1], rs, vo + 1024, so, 16);
  }
  publish(ws, WS_FH + (rt * 16 + j) * 128 + w * 8, tag);
  STAMP(5);
}

DI void a_phase1(LAS char* lds, unsigned ldsb, char* ws, const char* x, const char* wqkv, const char* w1, int rt, int j, int a, int lane, u64& epoch PROF_ARG) {
  { const int w = a; XDMA_SETUP(); XDMA(4); XDMA(5); XDMA(6); }
  STAMP(9);
  EPOCH_LOAD(epoch);
  const int r4 = lane >> 4, cp = lane & 15;
  unsigned vq[4];
#pragma unroll
  for (int ii = 0; ii < 4; ++ii) vq[ii] = (16 * a + 4 * ii + r4) * WQP + (cp ^ ((r4 << 2) | ii)) * 16;
  const bool actq = ((cp >> 2) ^ r4) < 3 || ((168 * j) & 63) > 24;
  const char* bq = wqkv + ((168 * j) & ~63);
  const char* bf = w1 + (7 * (j >> 1) + (j & 1) * 4) * 64;
  const unsigned vf = (a * 112 + (lane >> 2)) * W1P + (lane & 3) * 16;
#define SLABFILL(i, dst) do { _Pragma("unroll") for (int k = 0; k < 7; ++k) glds(bf + (i) * 64 + k * 16 * W1P, vf, ldsb + (dst) + a * 7168 + k * 1024); } while (0)
#define PANEL(f) do { _Pragma("unroll") for (int ii = 0; ii < 4; ++ii) glds(bq + (f) * 64 * WQP, vq[ii], ldsb + ((f) < 2 ? L_P01 + (f) * 16384 : ((f) - 2) * 16384) + a * 4096 + ii * 1024); } while (0)
  if (actq) { PANEL(0); PANEL(1); }
  WAITV(8); lds_signal(ldsb, C_XA + a);
  WAITV(0); lds_signal(ldsb, C_PAN + 0);
  STAMP(12);
  lds_wait(lds, C_XFREE, 4);
  STAMP(14);
  if (actq) { PANEL(2); PANEL(3); PANEL(4); PANEL(5); PANEL(6); }
  SLABFILL(0, L_S0);
  STAMP(15);
  WAITV(19); STAMP(16); lds_signal(ldsb, C_PAN + 1);
  WAITV(7); STAMP(17); lds_signal(ldsb, C_PAN + 2);
  WAITV(0); lds_signal(ldsb, C_LAND + 0);
  lds_wait(lds, C_REL + 1, 4); SLABFILL(1, L_S1);
  lds_wait(lds, C_REL + 2, 4); SLABFILL(2, L_S2);
  if (!(j & 1)) {
    lds_wait(lds, C_REL + 3, 4); SLABFILL(3, L_S3);
    WAITV(14); lds_signal(ldsb, C_LAND + 1);
    WAITV(7); lds_signal(ldsb, C_LAND + 2);
    WAITV(0); lds_signal(ldsb, C_LAND + 3);
  } else {
    WAITV(7); lds_signal(ldsb, C_LAND + 1);
    WAITV(0); lds_signal(ldsb, C_LAND + 2);
  }
#undef SLABFILL
}

#define EX(X, i) X[i] = __builtin_amdgcn_exp2f(X[i])
#define UNIT(CUR, NXT, KN, QN, VP, OP, VC, OC, DO_A, DO_P) UNIT_(CUR, NXT, KN, QN, VP, OP, VC, OC, DO_A, DO_P, false)
#define UNIT_(CUR, NXT, KN, QN, VP, OP, VC, OC, DO_A, DO_P, FIRST) do { \
    if (DO_A) NXT = MFMA(KN[0], QN[0], zero); \
    EX(CUR, 0); EX(CUR, 1); EX(CUR, 2); EX(CUR, 3); PIN(CUR); SBAR(); \
    if (DO_P) OP = MFMA(VP[1], __builtin_bit_cast(bf16x8, phi), OP); \
    EX(CUR, 4); EX(CUR, 5); plo[0] = pk2(CUR[0], CUR[1]); EX(CUR, 6); EX(CUR, 7); plo[1] = pk2(CUR[2], CUR[3]); PIN(CUR); PIN(plo); SBAR(); \
    if (DO_A) NXT = MFMA(KN[1], QN[1], NXT); \
    EX(CUR, 8); plo[2] = pk2(CUR[4], CUR[5]); EX(CUR, 9); plo[3] = pk2(CUR[6], CUR[7]); EX(CUR, 10); EX(CUR, 11); PIN(CUR); PIN(plo); SBAR(); \
    OC = MFMA(VC[0], __builtin_bit_cast(bf16x8, plo), (FIRST) ? zero : OC); \
    EX(CUR, 12); phi[0] = pk2(CUR[8], CUR[9]); EX(CUR, 13); phi[1] = pk2(CUR[10], CUR[11]); EX(CUR, 14); EX(CUR, 15); \
    phi[2] = pk2(CUR[12], CUR[13]); phi[3] = pk2(CUR[14], CUR[15]); PIN(phi); SBAR(); \
  } while (0)

DI void a_attn(LAS char* lds, unsigned ldsb, char* ws, int head, int qt, int a, int lane, u64 tag PROF_ARG) {
  const int hh = lane >> 5;
  const __amdgpu_buffer_rsrc_t rs = __builtin_amdgcn_make_buffer_rsrc(ws, 0, WS_END, 0x00020000);
  {
    const int jq = head / 3, jk = (16 + head) / 3, jv = (32 + head) / 3;
    const int rtl = lane < 32 ? 4 * a + ((lane >> 2) & 3) : qt, jl = lane < 16 ? jk : lane < 32 ? jv : jq;
    poll_flags(ws, WS_FQ + (rtl * 16 + jl) * 128 + (lane & 3) * 8, tag);
  }
  STAMP(2);
  const unsigned qp = WS_QKV + (head * 64 + qt * 4) * 2048, vo = lane * 16;
  const unsigned kp = WS_QKV + ((16 + head) * 64 + a * 16) * 2048, vp = WS_QKV + ((32 + head) * 64 + a * 16) * 2048;
#define LDF(p, t, s) LD_DS(rs, vo + (s) * 1024, (p) + (t) * 2048)
#define LDL(old, p, t, s) LDF(p, t, s)
  bf16x8 q0[2], q1[2], q2[2], q3[2], k0[2], k1[2], k2[2], k3[2], v0[2], v1[2], v2[2], v3[2];
  k0[0] = LDF(kp, 0, 0); k0[1] = LDF(kp, 0, 1); q0[0] = LDF(qp, (a + 0) & 3, 0); q0[1] = LDF(qp, (a + 0) & 3, 1); SBAR();
  q1[0] = LDF(qp, (a + 1) & 3, 0); q1[1] = LDF(qp, (a + 1) & 3, 1); SBAR();
  q2[0] = LDF(qp, (a + 2) & 3, 0); q2[1] = LDF(qp, (a + 2) & 3, 1); SBAR(); q3[0] = LDF(qp, (a + 3) & 3, 0); q3[1] = LDF(qp, (a + 3) & 3, 1); SBAR();
  v0[0] = LDF(vp, 0, 0); v0[1] = LDF(vp, 0, 1);
  k1[0] = LDF(kp, 1, 0); k1[1] = LDF(kp, 1, 1); v1[0] = LDF(vp, 1, 0); v1[1] = LDF(vp, 1, 1);
  k2[0] = LDF(kp, 2, 0); k2[1] = LDF(kp, 2, 1); v2[0] = LDF(vp, 2, 0); v2[1] = LDF(vp, 2, 1);
  k3[0] = LDF(kp, 3, 0); k3[1] = LDF(kp, 3, 1); v3[0] = LDF(vp, 3, 0); v3[1] = LDF(vp, 3, 1);
  SBAR();
  __builtin_amdgcn_s_setprio(2);
  const f32x16 zero = {};
  float mref[4];
  f32x16 o0, o1, o2, o3, sa, sb;
  o0 = MFMA(k0[0], q0[0], zero); o0 = MFMA(k0[1], q0[1], o0); o1 = MFMA(k0[0], q1[0], zero); o1 = MFMA(k0[1], q1[1], o1);
  o2 = MFMA(k0[0], q2[0], zero); o2 = MFMA(k0[1], q2[1], o2); o3 = MFMA(k0[0], q3[0], zero); o3 = MFMA(k0[1], q3[1], o3);
#define SETREF(s_, Q, i) do { \
    float m_ = fmaxf(fmaxf(s_[0], s_[1]), s_[2]), n_ = fmaxf(fmaxf(s_[3], s_[4]), s_[5]); \
    m_ = fmaxf(fmaxf(m_, s_[6]), s_[7]); n_ = fmaxf(fmaxf(n_, s_[8]), s_[9]); m_ = fmaxf(fmaxf(m_, s_[10]), s_[11]); n_ = fmaxf(fmaxf(n_, s_[12]), s_[13]); \
    m_ = fmaxf(fmaxf(m_, s_[14]), s_[15]); \
    m_ = xhalf_max(fmaxf(m_, n_)) + SHIFT; \
    const float h_ = rbf(m_), l_ = rbf(m_ - h_); mref[i] = h_ + l_; \
    u32x4 t_ = __builtin_bit_cast(u32x4, Q[1]); t_[2] = hh ? pk2(-h_, -l_) : t_[2]; Q[1] = __builtin_bit_cast(bf16x8, t_); } while (0)
  SETREF(o0, q0, 0);
  sa = MFMA(k0[0], q0[0], zero); sa = MFMA(k0[1], q0[1], sa);
  SETREF(o1, q1, 1); SETREF(o2, q2, 2); SETREF(o3, q3, 3);
  u32x4 plo, phi;
  SBAR();
  lds_signal(ldsb, C_AGO);
  STAMP(3);
#define TILE(KC, VC, VP, KN, T, PF, LAST) do { \
    UNIT(sa, sb, KC, q1, VP, o3, VC, o0, true, true); \
    if (PF) { VP[0] = LDL(VP[0], vp, (T) + 3, 0); VP[1] = LDL(VP[1], vp, (T) + 3, 1); SBAR(); } \
    UNIT(sb, sa, KC, q2, VC, o0, VC, o1, true, true); \
    UNIT(sa, sb, KC, q3, VC, o1, VC, o2, true, true); \
    if (PF && (T) + 4 < 16) { KC[0] = LDL(KC[0], kp, (T) + 4, 0); KC[1] = LDL(KC[1], kp, (T) + 4, 1); SBAR(); } \
    UNIT(sb, sa, KN, q0, VC, o2, VC, o3, !(LAST), true); \
  } while (0)
  {
    UNIT_(sa, sb, k0, q1, v0, o3, v0, o0, true, false, true);
    UNIT_(sb, sa, k0, q2, v0, o0, v0, o1, true, true, true);
    UNIT_(sa, sb, k0, q3, v0, o1, v0, o2, true, true, true);
    k0[0] = LDL(k0[0], kp, 4, 0); k0[1] = LDL(k0[1], kp, 4, 1); SBAR();
    UNIT_(sb, sa, k1, q0, v0, o2, v0, o3, true, true, true);
  }
#pragma unroll 1
  for (int t = 1; t < 13; t += 4) {
    const unsigned kp_ = kp, vp_ = vp;
    { const unsigned kp = kp_ + t * 2048, vp = vp_ + t * 2048;
      TILE(k1, v1, v0, k2, 0, true, false);
      TILE(k2, v2, v1, k3, 1, true, false);
      TILE(k3, v3, v2, k0, 2, true, false);
      UNIT(sa, sb, k0, q1, v3, o3, v0, o0, true, true);
      v3[0] = LDL(v3[0], vp, 6, 0); v3[1] = LDL(v3[1], vp, 6, 1); SBAR();
      UNIT(sb, sa, k0, q2, v0, o0, v0, o1, true, true);
      UNIT(sa, sb, k0, q3, v0, o1, v0, o2, true, true);
      if (t < 9) { k0[0] = LDL(k0[0], kp, 7, 0); k0[1] = LDL(k0[1], kp, 7, 1); } SBAR();
      UNIT(sb, sa, k1, q0, v0, o2, v0, o3, true, true);
    }
  }
  TILE(k1, v1, v0, k2, 13, false, false);
  TILE(k2, v2, v1, k3, 14, false, false);
  const unsigned gdone = CNT_LD(C_GDONE);
  TILE(k3, v3, v2, k0, 15, false, true);
  o3 = MFMA(v3[1], __builtin_bit_cast(bf16x8, phi), o3);
  STAMP(4);
  CNT_CHECK(gdone, C_GDONE, 4);
#define PUTO(O, i) do { const int d_ = (a + (i)) & 3; \
    *(LAS float*)(lds + L_MST + (d_ * 4 + (i)) * 256 + lane * 4) = hh ? O[12] : mref[i];     \
    if ((i) > 0) { LAS char* p_ = lds + L_MRG + (d_ * 3 + (i) - 1) * 4096 + lane * 16; \
      _Pragma("unroll") for (int g = 0; g < 4; ++g) *(LAS f32x4*)(p_ + g * 1024) = (f32x4){O[4 * g], O[4 * g + 1], O[4 * g + 2], O[4 * g + 3]}; } } while (0)
  PUTO(o0, 0); PUTO(o1, 1); PUTO(o2, 2); PUTO(o3, 3);
  WAITL(); lds_signal(ldsb, C_MRG);
  STAMP(7);
  lds_wait(lds, C_MRG, 4);
  STAMP(8);
  f32x16 o;
  bool bad = false;
  {
    float mw[4], lw[4]; f32x4 t[3][4];
#pragma unroll
    for (int i = 0; i < 4; ++i) { LAS const float* p = (LAS const float*)(lds + L_MST + (a * 4 + i) * 256) + (lane & 31); mw[i] = p[0]; lw[i] = p[32]; }
#pragma unroll
    for (int i = 0; i < 3; ++i)
#pragma unroll
      for (int g = 0; g < 4; ++g) t[i][g] = *(LAS const f32x4*)(lds + L_MRG + (a * 3 + i) * 4096 + lane * 16 + g * 1024);
    SBAR();
    float M = -INFINITY, f[4];
#pragma unroll
    for (int i = 0; i < 4; ++i) { bad |= !(lw[i] > 0x1p-70f && lw[i] < 0x1p90f); M = fmaxf(M, mw[i] + __builtin_amdgcn_logf(lw[i])); }
#pragma unroll
    for (int i = 0; i < 4; ++i) f[i] = __builtin_amdgcn_exp2f(mw[i] - M);
    float chk[4] = {};
#pragma unroll
    for (int e = 0; e < 16; ++e) {
      o[e] = fmaf(t[2][e >> 2][e & 3], f[3], fmaf(t[1][e >> 2][e & 3], f[2], fmaf(t[0][e >> 2][e & 3], f[1], o0[e] * f[0])));
      chk[e & 3] = fmaf(fabsf(o[e]), 0x1p-8f, chk[e & 3]);
    }
    bad |= !((chk[0] + chk[1]) + (chk[2] + chk[3]) < 0x1p100f);
  }
  if (__builtin_expect(__any(bad), 0)) {
    bf16x8 qa[2]; qa[0] = q0[0];
    u32x4 t_ = __builtin_bit_cast(u32x4, q0[1]); t_[2] = hh ? 0u : t_[2]; qa[1] = __builtin_bit_cast(bf16x8, t_);
    const unsigned kq = kp - a * 16 * 2048, vq = vp - a * 16 * 2048;
    float m = -INFINITY; o = zero;
#pragma unroll 1
    for (int t = 0; t < 64; ++t) {
      f32x16 s_ = MFMA(LDF(kq, t, 0), qa[0], zero); s_ = MFMA(LDF(kq, t, 1), qa[1], s_);
      float mx = s_[0];
#pragma unroll
      for (int e = 1; e < 16; ++e) mx = fmaxf(mx, s_[e]);
      const float mn = fmaxf(m, xhalf_max(mx));
      const float al = __builtin_amdgcn_exp2f(m - mn);
      m = mn;
#pragma unroll
      for (int e = 0; e < 16; ++e) { o[e] *= al; s_[e] = __builtin_amdgcn_exp2f(s_[e] - mn); }
      o = MFMA(LDF(vq, t, 0), __builtin_bit_cast(bf16x8, pack8(s_, 0)), o);
      o = MFMA(LDF(vq, t, 1), __builtin_bit_cast(bf16x8, pack8(s_, 1)), o);
    }
  }
  {
    const float rl = __builtin_amdgcn_rcpf(hi_half(o[12]));
#pragma unroll
    for (int e = 0; e < 16; ++e) o[e] *= rl;
    if (hh) { o[12] = 0.f; o[13] = 0.f; o[14] = 0.f; o[15] = 0.f; }
    const unsigned off = WS_O + ((qt * 4 + a) * 16 + head) * 2048 + lane * 16;
    ST_WT(pack8(o, 0), rs, off); ST_WT(pack8(o, 1), rs, off + 1024);
  }
  STAMP(5);
  publish(ws, WS_FO + (qt * 16 + head) * 128 + a * 8, tag);
  STAMP(6);
}

DI void g_phase2(LAS char* lds, unsigned ldsb, const char* x, const char* wo, const char* w2, char* ws, char* out, int rt64, int ct, int g, int lane, u64 tag PROF_ARG) {
  const int hh = lane >> 5, blk = (lane >> 4) & 1, q4 = (lane & 15) >> 2, p4 = lane & 3;
  const unsigned ring = L_G2 + g * 24576;
  const unsigned va = lane * 16;
  const unsigned vb0 = (lane >> 3) * XP + (((lane & 7) ^ (((lane >> 4) & 1) << 2)) * 16), vb1 = vb0 + 8 * XP;
  const char* hb0 = ws + WS_H + (size_t)((2 * rt64) * 56 + 14 * g) * 2048;
  const char* hb1 = hb0 + 56 * 2048;
  const char* wb = w2 + (size_t)(14 * g * 32) * XP + ct * 128;
  unsigned tb[2][2];
#pragma unroll
  for (int c = 0; c < 2; ++c)
#pragma unroll
    for (int t = 0; t < 2; ++t) tb[c][t] = (8 * t + 4 * hh + q4) * 128 + ((c ^ (q4 >> 1)) * 64) + blk * 32 + p4 * 8;
  bf16x8 ar[8][2];
#define LDA(dst, base) asm volatile("global_load_dwordx4 %0, %1, %2 sc1" : "=v"(dst) : "v"(va), "s"(base) : "memory")
#define ISSUE_B(st) do { glds(wb + (st) * 16 * XP, vb0, ldsb + ring + ((st) % 12) * 2048); glds(wb + (st) * 16 * XP, vb1, ldsb + ring + ((st) % 12) * 2048 + 1024); } while (0)
#define ISSUE_A(st) do { LDA(ar[(st) % 8][0], hb0 + (st) * 1024); LDA(ar[(st) % 8][1], hb1 + (st) * 1024); } while (0)
  const unsigned vw3 = (min(16 + 8 + (lane >> 3), DH - 1)) * XP + (((lane & 7) ^ (((lane >> 4) & 1) << 2)) * 16);
#define ISSUE_W(i) do { const char* b_ = wo + (size_t)((4 * g + (i)) * DH) * XP + ct * 128; const unsigned d_ = ldsb + ring + ((28 + 2 * (i)) % 12) * 2048; \
    glds(b_, vb0, d_); glds(b_, vb1, d_ + 1024); glds(b_ + 16 * XP, vb0, d_ + 2048); glds(b_, vw3, d_ + 3072); } while (0)
  lds_wait(lds, C_GDONE, 4);
  ISSUE_B(0); ISSUE_B(1); ISSUE_B(2); ISSUE_B(3); ISSUE_B(4); ISSUE_B(5); ISSUE_B(6);
  const unsigned fo = ((rt64 >> 1) * 16 + 4 * g + (lane & 3)) * 128 + (((2 * rt64) & 3) + ((lane >> 2) & 1)) * 8;
  poll_flags(ws, WS_FH + fo, tag);
  STAMP(6);
  ISSUE_A(0); ISSUE_A(1); ISSUE_A(2); ISSUE_A(3); ISSUE_A(4); ISSUE_A(5); ISSUE_A(6);
  f32x16 acc[2][2] = {};
  LAS const char* rp = lds + ring;
#define STEP(st, N) do { \
    asm volatile("s_waitcnt vmcnt(" #N ")" : "+v"(ar[(st) % 8][0]), "+v"(ar[(st) % 8][1]) :: "memory"); \
    LAS const char* b_ = rp + ((st) % 12) * 2048; \
    const bf16x8 w0 = cat(trd(b_ + tb[0][0]), trd(b_ + tb[0][1])), w1 = cat(trd(b_ + tb[1][0]), trd(b_ + tb[1][1])); \
    if ((st) + 7 < 28) { ISSUE_B((st) + 7); ISSUE_A((st) + 7); } else if ((st) + 7 < 32) { ISSUE_W((st) + 7 - 28); } \
    acc[0][0] = MFMA(w0, ar[(st) % 8][0], acc[0][0]); acc[0][1] = MFMA(w1, ar[(st) % 8][0], acc[0][1]); \
    acc[1][0] = MFMA(w0, ar[(st) % 8][1], acc[1][0]); acc[1][1] = MFMA(w1, ar[(st) % 8][1], acc[1][1]); \
    SBAR(); } while (0)
  STEP(0, 12); STEP(1, 14); STEP(2, 16); STEP(3, 18); STEP(4, 20); STEP(5, 22); STEP(6, 24); STEP(7, 24); STEP(8, 24); STEP(9, 24);
  STEP(10, 24); STEP(11, 24); STEP(12, 24); STEP(13, 24); STEP(14, 24); STEP(15, 24); STEP(16, 24); STEP(17, 24); STEP(18, 24); STEP(19, 24);
  STEP(20, 24); STEP(21, 24); STEP(22, 24); STEP(23, 24); STEP(24, 24); STEP(25, 24); STEP(26, 20); STEP(27, 16);
  WAITV(0);
  bf16x8 wf[8][2];
#pragma unroll
  for (int k = 0; k < 8; ++k)
#pragma unroll
    for (int c = 0; c < 2; ++c) { LAS const char* b_ = rp + ((28 + k) % 12) * 2048; wf[k][c] = cat(trd(b_ + tb[c][0]), trd(b_ + tb[c][1])); }
  const int tok0 = rt64 * 64 + g * 16 + (lane >> 3);
  const size_t xo = (size_t)tok0 * XP + ct * 128 + (lane & 7) * 16;
  const u32x4 xres0 = *(const u32x4*)(x + xo), xres1 = *(const u32x4*)(x + xo + 8 * XP);
  STAMP(7);
  poll_flags(ws, WS_FO + fo, tag);
  STAMP(8);
  {
    const __amdgpu_buffer_rsrc_t rs = __builtin_amdgcn_make_buffer_rsrc(ws, 0, WS_END, 0x00020000);
    const unsigned ob = WS_O + ((2 * rt64) * 16 + 4 * g) * 2048;
    bf16x8 of[2][8];
#pragma unroll
    for (int k = 0; k < 8; ++k) { of[0][k] = LD_DS(rs, va, ob + k * 1024); of[1][k] = LD_DS(rs, va, ob + 16 * 2048 + k * 1024); }
    SBAR();
#pragma unroll
    for (int k = 0; k < 8; ++k)
#pragma unroll
      for (int a = 0; a < 2; ++a)
#pragma unroll
        for (int c = 0; c < 2; ++c) acc[a][c] = MFMA(wf[k][c], of[a][k], acc[a][c]);
  }
  {
    LAS char* d = lds + ring + (lane & 31) * 272 + hh * 16;
#pragma unroll
    for (int a = 0; a < 2; ++a)
#pragma unroll
      for (int c = 0; c < 2; ++c)
#pragma unroll
        for (int gq = 0; gq < 4; ++gq)
          *(LAS f32x4*)(d + a * 32 * 272 + c * 128 + gq * 32) = (f32x4){acc[a][c][4 * gq], acc[a][c][4 * gq + 1], acc[a][c][4 * gq + 2], acc[a][c][4 * gq + 3]};
  }
  WAITL(); lds_signal(ldsb, C_RED);
  lds_wait(lds, C_RED, 4);
#pragma unroll
  for (int it = 0; it < 2; ++it) {
    const u32x4 xr = it ? xres1 : xres0;
    float v[8];
#pragma unroll
    for (int e = 0; e < 4; ++e) { v[2 * e] = __uint_as_float(xr[e] << 16); v[2 * e + 1] = __uint_as_float(xr[e] & 0xffff0000u); }
    LAS const char* sp = lds + L_G2 + (g * 16 + it * 8 + (lane >> 3)) * 272 + (lane & 7) * 32;
#pragma unroll
    for (int s = 0; s < 4; ++s) {
      const f32x4 t0 = *(LAS const f32x4*)(sp + s * 24576), t1 = *(LAS const f32x4*)(sp + s * 24576 + 16);
#pragma unroll
      for (int e = 0; e < 4; ++e) { v[e] += t0[e]; v[4 + e] += t1[e]; }
    }
    *(u32x4*)(out + xo + it * 8 * XP) = (u32x4){pk2(v[0], v[1]), pk2(v[2], v[3]), pk2(v[4], v[5]), pk2(v[6], v[7])};
  }
  STAMP(9);
}

__global__ void __launch_bounds__(512, 2) fused_block(const char* __restrict__ x, const char* __restrict__ wqkv, const char* __restrict__ wo,
                                                      const char* __restrict__ w1, const char* __restrict__ w2, char* out, char* ws) {
  u64 pc0; asm volatile("s_getpc_b64 %0" : "=s"(pc0));
  extern __shared__ __attribute__((aligned(16))) char smem[];
  LAS char* lds = (LAS char*)smem;
  const unsigned ldsb = (unsigned)(uintptr_t)lds;
  const int lane = threadIdx.x & 63, wave = __builtin_amdgcn_readfirstlane(threadIdx.x >> 6);
  const int xcd = blockIdx.x & 7, slot = blockIdx.x >> 3;
  STAMP(0);
  if (threadIdx.x < 32) ((LAS unsigned*)(lds + L_CNT))[threadIdx.x] = 0u;
  BARRIER_L();
  u64 epoch;
#define RPX 4
  const int rt = RPX * (xcd % (16 / RPX)) + (slot % RPX), j = (32 / RPX) * (xcd / (16 / RPX)) + slot / RPX;
  if (wave < 4) {
    g_phase1(lds, ldsb, x, ws, rt, j, wave, lane, epoch PROF_PASS);
    const u64 tag = epoch + 1;
    int lane2 = lane; OPAQUE(lane2);
    if (slot < 28) g_phase2(
lds, ldsb, x, wo, w2, ws, out, 4 * xcd + slot / 7, slot % 7, wave, lane2, tag PROF_PASS);
    else if (blockIdx.x == 255 && wave == 0) {
#pragma unroll
      for (int i = 0; i < 4; ++i) poll_flags(ws, WS_FQ + (i * 64 + lane) * 128, tag);
      if (lane == 0) __hip_atomic_store((GAS u64*)(ws + WS_EPOCH), tag, RLX_AGENT);
    }
  } else {
    u32x4 pfc = {}; unsigned pft = 0;
    a_phase1(lds, ldsb, ws, x, wqkv, w1, rt, j, wave - 4, lane, epoch PROF_PASS);
    asm volatile("" :: "v"(pfc), "v"(pft));
    STAMP(1);
    a_attn(lds, ldsb, ws, 2 * xcd + (slot >> 4), slot & 15, wave - 4, lane, epoch + 1 PROF_PASS);
  }
}

extern "C" void kernel_launch(void* const* d_in, const int* in_sizes, int n_in, void* d_out, int out_size, void* d_ws, size_t ws_size, hipStream_t stream) {
  (void)in_sizes; (void)n_in; (void)out_size; (void)ws_size;
  (void)hipFuncSetAttribute((const void*)fused_block, hipFuncAttributeMaxDynamicSharedMemorySize, LDS_BYTES);
  fused_block<<<256, 512, LDS_BYTES, stream>>>((const char*)d_in[0], (const char*)d_in[1], (const char*)d_in[2], (const char*)d_in[4], (const char*)d_in[5], (char*)d_out, (char*)d_ws);
}
```

```cpp
#include <hip/hip_runtime.h>
#include <hip/hip_bf16.h>
#include <stdint.h>

typedef short bf16x8 __attribute__((ext_vector_type(8)));
typedef short s16x4 __attribute__((ext_vector_type(4)));
typedef float f32x16 __attribute__((ext_vector_type(16)));
typedef float f32x4 __attribute__((ext_vector_type(4)));
typedef unsigned u32x4 __attribute__((ext_vector_type(4)));
typedef __bf16 bf16x2 __attribute__((ext_vector_type(2)));
typedef unsigned long long u64;
#define GAS __attribute__((address_space(1)))
#define LAS __attribute__((address_space(3)))
#define DI __device__ __forceinline__
#define MFMA(a, b, c) __builtin_amdgcn_mfma_f32_32x32x16_bf16((a), (b), (c), 0, 0, 0)
#define SBAR() __builtin_amdgcn_sched_barrier(0)
#define PIN(x) asm volatile("" : "+v"(x))
#define RLX_AGENT __ATOMIC_RELAXED, __HIP_MEMORY_SCOPE_AGENT
#define WAITV(n) asm volatile("s_waitcnt vmcnt(" #n ")" ::: "memory")
#define WAITL() asm volatile("s_waitcnt lgkmcnt(0)" ::: "memory")
#define BARRIER() asm volatile("s_barrier" ::: "memory")
#define BARRIER_L() asm volatile("s_waitcnt lgkmcnt(0)\n\ts_barrier" ::: "memory")

constexpr int S = 2048, D = 448, H = 16, DH = 28, F = 1792;
constexpr int XP = D * 2;
constexpr int WQP = 3 * D * 2;
constexpr int W1P = F * 2;
constexpr float QSCALE = 0.18898223650461362f * 1.4426950408889634f;
constexpr float SHIFT = 100.f;
constexpr float LMIN = 0x1p-101f;

constexpr int WS_EPOCH = 0;
constexpr int WS_END0 = 16 << 20;
constexpr int FW = 128, FG = 4 * FW;
constexpr int WS_FQ = WS_END0;
constexpr int WS_FH = 576 * 1024;
constexpr int WS_FO = WS_FH + 256 * 512;
constexpr int WS_QKV = 1 << 20;
constexpr int WS_H = WS_QKV + 48 * 64 * 2048;
constexpr int WS_O = WS_H + 64 * 56 * 2048;
constexpr int WS_END = WS_O + 64 * 16 * 2048;

constexpr int L_P01 = 114688, L_S0 = 81920, L_S1 = 0;
constexpr int L_VT = 147456;
constexpr int L_G2 = 0;
constexpr int L_MRG = 98304;
constexpr int L_MST = 147456;
constexpr int L_CNT = 159744;
constexpr int LDS_BYTES = 163840;
constexpr unsigned SPIN_CAP = 1u << 22;
constexpr int PD = 3;
constexpr int PUBST = 13;

DI unsigned pk2(float lo, float hi) { bf16x2 r = {(__bf16)lo, (__bf16)hi}; return __builtin_bit_cast(unsigned, r); }
DI float rbf(float v) { return (float)(__bf16)v; }
DI float dot2(unsigned a, unsigned b, float c) { return __builtin_amdgcn_fdot2_f32_bf16(__builtin_bit_cast(bf16x2, a), __builtin_bit_cast(bf16x2, b), c, false); }
DI float xhalf_max(float m) { auto r = __builtin_amdgcn_permlane32_swap(__float_as_uint(m), __float_as_uint(m), false, false); return fmaxf(__uint_as_float(r[0]), __uint_as_float(r[1])); }
DI float xhalf_sum(float m) { auto r = __builtin_amdgcn_permlane32_swap(__float_as_uint(m), __float_as_uint(m), false, false); return __uint_as_float(r[0]) + __uint_as_float(r[1]); }
DI float hi_half(float m) { auto r = __builtin_amdgcn_permlane32_swap(__float_as_uint(m), __float_as_uint(m), false, false); return __uint_as_float(r[1]); }
DI s16x4 trd(LAS const char* p) { typedef short v4s __attribute__((ext_vector_type(4))); return __builtin_bit_cast(s16x4, __builtin_amdgcn_ds_read_tr16_b64_v4i16((LAS v4s*)p)); }
DI bf16x8 cat(s16x4 a, s16x4 b) { return __builtin_shufflevector(a, b, 0, 1, 2, 3, 4, 5, 6, 7); }
DI void glds(const void* sbase, unsigned voff, unsigned lds_dst) {
  unsigned keep;
  asm volatile("s_mov_b32 %0, m0\n\ts_mov_b32 m0, %3\n\ts_nop 0\n\tglobal_load_lds_dwordx4 %1, %2\n\ts_mov_b32 m0, %0"
               : "=&s"(keep) : "v"(voff), "s"(sbase), "s"(lds_dst) : "memory");
}
DI void lds_signal(unsigned ldsb, int idx) {
  const unsigned ad = ldsb + L_CNT + idx * 4, one = 1u;
  asm volatile("s_mov_b64 exec, 1\n\tds_add_u32 %0, %1\n\ts_mov_b64 exec, -1" :: "v"(ad), "v"(one) : "memory");
}
template <int SLEEP = 1>
DI void lds_wait(LAS char* lds, int idx, unsigned n) {
#pragma unroll 1
  for (unsigned sp = 0; sp < SPIN_CAP; ++sp) {
    unsigned v = __builtin_amdgcn_readfirstlane(__hip_atomic_load((LAS unsigned*)(lds + L_CNT) + idx, __ATOMIC_RELAXED, __HIP_MEMORY_SCOPE_WORKGROUP));
    if (v >= n) break;
    if (SLEEP) __builtin_amdgcn_s_sleep(SLEEP);
  }
  asm volatile("" ::: "memory");
}
template <int SLEEP = 2>
DI void poll_flags(char* ws, unsigned off, u64 tag) {
  GAS u64* p = (GAS u64*)(ws + off);
#pragma unroll 1
  for (unsigned sp = 0; sp < SPIN_CAP; ++sp) {
    u64 v = __hip_atomic_load(p, RLX_AGENT);
    if (__all(v == tag)) break;
    if (SLEEP) __builtin_amdgcn_s_sleep(SLEEP);
  }
  __builtin_amdgcn_fence(__ATOMIC_ACQUIRE, "wavefront");
  asm volatile("" ::: "memory");
}
DI void publish(char* ws, unsigned off, u64 tag) {
  WAITV(0);
  if ((threadIdx.x & 63) == 0) __hip_atomic_store((GAS u64*)(ws + off), tag, RLX_AGENT);
}
#define CNT_LD(idx) __hip_atomic_load((LAS unsigned*)(lds + L_CNT) + (idx), __ATOMIC_RELAXED, __HIP_MEMORY_SCOPE_WORKGROUP)
#define CNT_CHECK(v, idx, n) do { if (__builtin_expect(__builtin_amdgcn_readfirstlane(v) < (n), 0)) lds_wait(lds, (idx), (n)); asm volatile("" ::: "memory"); } while (0)
DI u32x4 pack8(const f32x16& a, int s) {
  return (u32x4){pk2(a[8 * s], a[8 * s + 1]), pk2(a[8 * s + 2], a[8 * s + 3]), pk2(a[8 * s + 4], a[8 * s + 5]), pk2(a[8 * s + 6], a[8 * s + 7])};
}
#define LD_DS(rsrc, voff, soff) __builtin_bit_cast(bf16x8, __builtin_amdgcn_raw_buffer_load_b128((rsrc), (voff), (soff), 16))
#define ST_WT(v, rsrc, off) __builtin_amdgcn_raw_buffer_store_b128((v), (rsrc), (off), 0, 16)

enum { C_AGO = 0, C_GDONE = 2, C_MRG = 3, C_XF1 = 5, C_XF2 = 6, C_REL = 8, C_LAND = 12, C_PAN = 16, C_RED = 24 };

DI u32x4 center8(u32x4 w, float negm) {
  float t0, t1, t2, t3, t4, t5, t6, t7; u32x4 r;
  asm("v_dot2_f32_bf16 %4, %12, %17, %16\n\tv_dot2_f32_bf16 %5, %12, %18, %16\n\t"
      "v_dot2_f32_bf16 %6, %13, %17, %16\n\tv_dot2_f32_bf16 %7, %13, %18, %16\n\t"
      "v_dot2_f32_bf16 %8, %14, %17, %16\n\tv_dot2_f32_bf16 %9, %14, %18, %16\n\t"
      "v_dot2_f32_bf16 %10, %15, %17, %16\n\tv_dot2_f32_bf16 %11, %15, %18, %16\n\t"
      "v_cvt_pk_bf16_f32 %0, %4, %5\n\tv_cvt_pk_bf16_f32 %1, %6, %7\n\tv_cvt_pk_bf16_f32 %2, %8, %9\n\tv_cvt_pk_bf16_f32 %3, %10, %11"
      : "=&v"(r[0]), "=&v"(r[1]), "=&v"(r[2]), "=&v"(r[3]), "=&v"(t0), "=&v"(t1), "=&v"(t2), "=&v"(t3), "=&v"(t4), "=&v"(t5), "=&v"(t6), "=&v"(t7)
      : "v"(w[0]), "v"(w[1]), "v"(w[2]), "v"(w[3]), "v"(negm), "s"(0x00003f80u), "s"(0x3f800000u));
  return r;
}
DI unsigned max2(unsigned u, short f) {
  typedef short s16x2 __attribute__((ext_vector_type(2)));
  return __builtin_bit_cast(unsigned, __builtin_elementwise_max(__builtin_bit_cast(s16x2, u), (s16x2){f, f}));
}
#define EPOCH_LOAD(e) asm volatile("s_load_dwordx2 %0, %1, 0x0\n\ts_waitcnt lgkmcnt(0)" : "=s"(e) : "s"(ws + WS_EPOCH) : "memory")
DI s16x4 trd_a(unsigned a) { return trd((LAS const char*)(uintptr_t)a); }
#define OPAQUE(x) asm volatile("" : "+v"(x))

DI int phys(int p, int rot) { const int q = p + rot; return q >= 7 ? q - 7 : q; }
DI bool ffn_short(int j) { return (j & 3) == 3 - (j >> 2); }
DI int ffn_first(int j) { return 14 * (j >> 2) + 4 * (j & 3) - ((j & 3) > 3 - (j >> 2) ? 2 : 0); }
#define SPANEL_SETUP(w) const unsigned svf = (16 * (w) + (lane >> 3)) * W1P + (((lane & 7) ^ (((lane >> 4) & 1) << 2)) * 16)
#define SPANEL(src, f, dst, w) do { const char* s_ = (src) + phys(f, rot) * 64 * W1P; const unsigned d_ = ldsb + (dst) + (f) * 8192 + (w) * 2048; \
    glds(s_, svf, d_); glds(s_ + 8 * W1P, svf, d_ + 1024); } while (0)
#define PANEL_SETUP(w) unsigned vq[4]; \
    _Pragma("unroll") for (int ii = 0; ii < 4; ++ii) vq[ii] = (16 * (w) + 4 * ii + (lane >> 4)) * WQP + ((lane & 15) ^ (((lane >> 4) << 2) | ii)) * 16; \
    const bool actq = (((lane & 15) >> 2) ^ (lane >> 4)) < 3 || ((168 * j) & 63) > 24; \
    const char* bq = wqkv + ((168 * j) & ~63)
#define PANEL(f, w) do { _Pragma("unroll") for (int ii = 0; ii < 4; ++ii) glds(bq + phys(f, rot) * 64 * WQP, vq[ii], ldsb + ((f) < 2 ? L_P01 + (f) * 16384 : ((f) - 2) * 16384) + (w) * 4096 + ii * 1024); } while (0)
#define XDMA_SETUP() const char* xb = x + (size_t)(rt * 128 + w * 32) * XP; \
    const unsigned v0 = (lane >> 3) * XP + (((lane & 7) ^ ((lane >> 4) & 3)) * 16), v1 = (v0 ^ 64) + 8 * XP
#define XDMA(p) do { const unsigned d_ = ldsb + (p) * 16384 + w * 4096; const char* s_ = xb + phys(p, rot) * 128; \
    glds(s_, v0, d_); glds(s_, v1, d_ + 1024); glds(s_ + 16 * XP, v0, d_ + 2048); glds(s_ + 16 * XP, v1, d_ + 3072); } while (0)

DI void g_phase1(LAS char* lds, unsigned ldsb, const char* x, char* ws, int rt, int j, int rot, int w, int lane, u64& epoch) {
  const int rb = rt * 4 + w;
  const int r = lane & 31, hh = lane >> 5;
  bf16x8 xf[28];
  float rstd;
  {
    XDMA_SETUP();
 XDMA(0);
    XDMA(1); XDMA(2); XDMA(3); XDMA(4); XDMA(5); XDMA(6);
    BARRIER();
    EPOCH_LOAD(epoch);
    unsigned sw[4];
#pragma unroll
    for (int s = 0; s < 4; ++s) sw[s] = ldsb + w * 4096 + r * 128 + ((2 * s + hh) ^ ((r >> 1) & 7)) * 16;
    float sm[4] = {}, sq[4] = {};
#define XREAD(p) do { _Pragma("unroll") for (int s = 0; s < 4; ++s) xf[4 * (p) + s] = *(LAS const bf16x8*)(uintptr_t)(sw[s] + ((p) & 3) * 16384); } while (0)
#define XSUM(p) do { _Pragma("unroll") for (int s = 0; s < 4; ++s) { const u32x4 u_ = __builtin_bit_cast(u32x4, xf[4 * (p) + s]); \
      _Pragma("unroll") for (int d = 0; d < 4; ++d) { sm[d] = dot2(u_[d], 0x3f803f80u, sm[d]); sq[d] = dot2(u_[d], u_[d], sq[d]); } } } while (0)

    WAITV(20);
    XREAD(0); XREAD(1); XSUM(0); SBAR(); WAITV(16); XREAD(2); XSUM(1); SBAR(); WAITV(12); XREAD(3);
    lds_signal(ldsb, C_XF1);
    XSUM(2); SBAR();
#pragma unroll
    for (int s = 0; s < 4; ++s) { sw[s] += 65536; OPAQUE(sw[s]); }

    WAITV(8); XREAD(4); XSUM(3); SBAR(); WAITV(4); XREAD(5); XSUM(4); SBAR(); WAITV(0); XREAD(6);
    lds_signal(ldsb, C_XF2);
    XSUM(5); XSUM(6);

    const float mean = xhalf_sum((sm[0] + sm[1]) + (sm[2] + sm[3])) * (1.0f / D);
    const float msq = xhalf_sum((sq[0] + sq[1]) + (sq[2] + sq[3])) * (1.0f / D);
    const float negm = -rbf(mean);
    rstd = __builtin_amdgcn_rcpf(rbf(__builtin_amdgcn_sqrtf(rbf(rbf(fmaxf(fmaf(-mean, mean, msq), 0.f)) + 1e-5f))));

#pragma unroll
    for (int s = 0; s < 28; ++s) xf[s] = __builtin_bit_cast(bf16x8, center8(__builtin_bit_cast(u32x4, xf[s]), negm));
  }
  const u64 tag = epoch + 1;
  const __amdgpu_buffer_rsrc_t rs = __builtin_amdgcn_make_buffer_rsrc(ws, 0, WS_END, 0x00020000);
  const int c8 = 32 * ((lane >> 4) & 1) + 8 * (lane & 3), q4 = (lane & 15) >> 2;
  const int key0 = (q4 << 2) | (2 * hh), off0 = (168 * j) & 63;
  const int fb0 = ffn_first(j);
  const bool shrt = ffn_short(j);
  const unsigned vo = lane * 16;
  LAS char* vim_w = lds + L_VT + w * 2304 + r * 72 + hh * 8;
  LAS const char* vim_r = lds + L_VT + w * 2304 + (4 * hh + q4) * 72 + c8;
  u32x4 f0, f1, hp[4][2]; float sc;
#define EPI(X, cp, k) do { \
    const int b_ = 3 * j + (cp); \
    if ((k) == 0) { sc = ((cp) < 3 && b_ < 16) ? rstd * QSCALE : rstd; X[0] *= sc; X[1] *= sc; X[2] *= sc; X[3] *= sc; } \
    if ((k) == 1) { X[4] *= sc; X[5] *= sc; X[6] *= sc; X[7] *= sc; f0[0] = pk2(X[0], X[1]); f0[1] = pk2(X[2], X[3]); } \
    if ((k) == 2) { X[8] *= sc; X[9] *= sc; X[10] *= sc; X[11] *= sc; f0[2] = pk2(X[4], X[5]); f0[3] = pk2(X[6], X[7]); } \
    if ((k) == 3) { X[12] *= sc; X[13] *= sc; X[14] *= sc; X[15] *= sc; f1[0] = pk2(X[8], X[9]); f1[1] = pk2(X[10], X[11]); } \
    if ((k) == 4) { f1[2] = pk2(X[12], X[13]); f1[3] = pk2(X[14], X[15]); \
      if ((cp) < 3) {   \
        f1[2] = hh ? (b_ < 16 ? 0u : b_ < 32 ? 0x3f803f80u : 0x00003f80u) : f1[2]; f1[3] = hh ? 0u : f1[3]; } \
      else { f0[0] = max2(f0[0], 0); f0[1] = max2(f0[1], 0); f0[2] = max2(f0[2], 0); f0[3] = max2(f0[3], 0); } } \
    if ((k) == 5) { \
      if ((cp) >= 3) { f1[0] = max2(f1[0], 0); f1[1] = max2(f1[1], 0); f1[2] = max2(f1[2], 0); f1[3] = max2(f1[3], 0); } \
      else if (b_ >= 32) {     \
        typedef unsigned u32x2 __attribute__((ext_vector_type(2))); \
        *(LAS u32x2*)(vim_w) = (u32x2){f0[0], f0[1]}; *(LAS u32x2*)(vim_w + 16) = (u32x2){f0[2], f0[3]}; \
        *(LAS u32x2*)(vim_w + 32) = (u32x2){f1[0], f1[1]}; *(LAS u32x2*)(vim_w + 48) = (u32x2){f1[2], f1[3]}; \
        f0 = __builtin_bit_cast(u32x4, cat(trd(vim_r), trd(vim_r + 576))); f1 = __builtin_bit_cast(u32x4, cat(trd(vim_r + 1152), trd(vim_r + 1728))); } } \
    if ((k) == 8) { \
      if ((cp) < 3) { const unsigned so_ = WS_QKV + (b_ * 64 + rb) * 2048; \
        __builtin_amdgcn_raw_buffer_store_b128(f0, rs, vo, so_, 16); __builtin_amdgcn_raw_buffer_store_b128(f1, rs, vo + 1024, so_, 16); } \
      else { hp[(cp) < 3 ? 0 : (cp) - 3][0] = f0; hp[(cp) < 3 ? 0 : (cp) - 3][1] = f1; } } \
    if ((k) < 4) PIN(X); \
    if ((k) >= 1 && (k) < 5) { PIN(f0); PIN(f1); } } while (0)
  unsigned qa0, qa1;
  const unsigned fa = ldsb + (8 * hh + q4) * 128 + (q4 >> 1) * 64 + c8;
#define QADDR(c) do { const int cb_ = off0 + 56 * (c) + c8; qa0 = ldsb + L_P01 + (8 * hh + q4) * 256 + (((cb_ >> 4) ^ key0) << 4) + (cb_ & 8); qa1 = (qa0 + 1024) ^ 16; OPAQUE(qa0); OPAQUE(qa1); } while (0)
#define QOFF(st) ((st) < 8 ? (st) * 4096 : (((st) - 8) & 15) * 4096)
#define FRAG(c, st) ((c) < 3 ? cat(trd_a(qa0 + QOFF(st)), trd_a(qa1 + QOFF(st))) : cat(trd_a(fb + (st) * 2048), trd_a(fb + (st) * 2048 + 512)))
  __builtin_amdgcn_s_setprio(2);

  lds_wait<0>(lds, C_PAN + 0, 4);

  bf16x8 wf[8];
  unsigned fb = fa;
  QADDR(0);
#pragma unroll
  for (int st = 0; st < PD; ++st) wf[st] = FRAG(0, st);
  f32x16 A, B;
  const f32x16 zero = {};
  f0 = (u32x4){}; f1 = (u32x4){};
  unsigned pubm = 0;
#define WFI(c, st) ((((c) * 28) + (st)) & 7)
#define SWEEP(c, X, Y, HAS_NEXT) do { \
    unsigned landed_ = 0; \
    _Pragma("unroll") for (int st = 0; st < 28; ++st) { \
      if ((c) == 0 && (st == 0 || st == 8)) landed_ = CNT_LD(C_PAN + 1 + st / 8); \
      if ((c) == 0 && (st == 8 - PD || st == 16 - PD)) CNT_CHECK(landed_, C_PAN + 1 + st / 8, 4);     \
      if ((c) < 3 && st == 8 - PD) { qa0 -= L_P01; qa1 -= L_P01; OPAQUE(qa0); OPAQUE(qa1); } \
      if ((c) < 3 && st == 24 - PD) { qa0 += 65536; qa1 += 65536; OPAQUE(qa0); OPAQUE(qa1); } \
      if ((c) == 3 && st == 12 - PD) landed_ = CNT_LD(C_LAND + 1); \
      if ((c) == 3 && st == 16 - PD) CNT_CHECK(landed_, C_LAND + 1, 4);                               \
      if (HAS_NEXT && ((c) == 2 || (c) == 4) && st == 24 - PD) landed_ = CNT_LD(C_LAND + (c) - 2); \
      if (HAS_NEXT && st == 28 - PD) { \
        if ((c) < 2) QADDR((c) + 1); \
        else { if ((c) == 2 || (c) == 4) CNT_CHECK(landed_, C_LAND + (c) - 2, 4); \
               fb = (fa + ((c) < 4 ? L_S0 : L_S1)) ^ (((c) & 1) * 64); OPAQUE(fb); } } \
      if (st < 28 - PD) wf[WFI(c, st + PD)] = FRAG(c, st + PD); else if (HAS_NEXT) wf[WFI(c, st + PD)] = FRAG((c) + 1, st + PD - 28); \
      X = MFMA(wf[WFI(c, st)], xf[st], st == 0 ? zero : X); \
      if ((c) > 0 && st >= 1 && st <= 9) EPI(Y, (c) - 1, st - 1); \
      SBAR(); \
      if ((c) == 2 && (st == 7 || st == 23)) lds_signal(ldsb, C_REL + st / 8);     \
      if ((c) == 3 && st >= PUBST) {     \
        if (st == 27 && !pubm) WAITV(0); \
        unsigned v_; asm volatile("s_getreg_b32 %0, hwreg(HW_REG_IB_STS, 0, 4)" : "=s"(v_) :: "memory");     \
        if (__builtin_expect((v_ | pubm) == 0, 0)) { pubm = 64u; \
          if (lane < 3) __hip_atomic_store((GAS u64*)(ws + WS_FQ + ((3 * j + lane) * 64 + rb) * 128), tag, RLX_AGENT); } } \
    } } while (0)
#define EPI_ALL(X, cp) do { EPI(X, cp, 0); EPI(X, cp, 1); EPI(X, cp, 2); EPI(X, cp, 3); EPI(X, cp, 4); EPI(X, cp, 5); EPI(X, cp, 8); } while (0)
  SWEEP(0, A, B, true);
  SWEEP(1, B, A, true);
  SWEEP(2, A, B, true);
  SWEEP(3, B, A, true);
  if (!shrt) {
    SWEEP(4, A, B, true);
    SWEEP(5, B, A, true);
    SWEEP(6, A, B, false);
    lds_signal(ldsb, C_GDONE);
    EPI_ALL(A, 6);
  } else {
    SWEEP(4, A, B, false);
    lds_signal(ldsb, C_GDONE);
    EPI_ALL(A, 4);
    hp[2][0] = f0; hp[2][1] = f1; hp[3][0] = f0; hp[3][1] = f1;
  }

  lds_wait(lds, C_AGO, 4);

#pragma unroll
  for (int c = 0; c < 4; ++c) {
    const unsigned so = (c >= 2 && shrt) ? 0x7ff00000u : WS_H + (rb * 56 + fb0 + c) * 2048;
    __builtin_amdgcn_raw_buffer_store_b128(hp[c][0], rs, vo, so, 16); __builtin_amdgcn_raw_buffer_store_b128(hp[c][1], rs, vo + 1024, so, 16);
  }
  __builtin_amdgcn_s_setprio(0);
  publish(ws, WS_FH + (rt * 16 + j) * FG + w * FW, tag);
}

DI void a_phase1(LAS char* lds, unsigned ldsb, char* ws, const char* wqkv, const char* w1, int j, int rot, int a, int lane, u64& epoch, unsigned pfc) {
  PANEL_SETUP(a);
  const char* bf = w1 + ffn_first(j) * 64;
  SPANEL_SETUP(a);
  if (lane < 32) ((LAS unsigned*)(lds + L_CNT))[lane] = 0u;
  BARRIER_L();
  if (actq) { PANEL(0, a); PANEL(1, a); }
  EPOCH_LOAD(epoch);
  WAITV(0); lds_signal(ldsb, C_PAN + 0);
  lds_wait<1>(lds, C_XF1, 4);

  if (actq) { PANEL(2, a); PANEL(3, a); PANEL(4, a); PANEL(5, a); }
  lds_wait<0>(lds, C_XF2, 4);

  if (actq) { PANEL(6, a); }
  WAITV(12); lds_signal(ldsb, C_PAN + 1);
  SPANEL(bf, 0, L_S0, a); SPANEL(bf, 1, L_S0, a); SPANEL(bf, 2, L_S0, a); SPANEL(bf, 3, L_S0, a);

  WAITV(8); lds_signal(ldsb, C_PAN + 2);
  const bool shrt = ffn_short(j);
  u32x4 s0[3][2], s1[7][2];
#define SLOAD(dst, src, f) do { const char* s_ = (src) + phys(f, rot) * 64 * W1P + svf; dst[0] = *(const u32x4*)s_; dst[1] = *(const u32x4*)(s_ + 8 * W1P); } while (0)
#define SPUT(off, v) do { *(LAS u32x4*)(lds + (off) + lane * 16) = v[0]; *(LAS u32x4*)(lds + (off) + 1024 + lane * 16) = v[1]; } while (0)
#pragma unroll
  for (int f = 0; f < 3; ++f) SLOAD(s0[f], bf, 4 + f);
  WAITV(6); lds_signal(ldsb, C_LAND + 0);
  if (!shrt) {
#pragma unroll
    for (int f = 0; f < 7; ++f) SLOAD(s1[f], bf + 128, f);
  }
  lds_wait(lds, C_REL + 0, 4);
#pragma unroll
  for (int f = 0; f < 3; ++f) SPUT(L_S0 + (4 + f) * 8192 + a * 2048, s0[f]);
  lds_signal(ldsb, C_LAND + 1);
  if (!shrt) {
    lds_wait(lds, C_REL + 2, 4);
#pragma unroll
    for (int f = 0; f < 7; ++f) SPUT(L_S1 + f * 8192 + a * 2048, s1[f]);
    lds_signal(ldsb, C_LAND + 2);
  }
  WAITV(0);
  asm volatile("" :: "v"(pfc));
}

#define EX(X, i) do { float t_ = __builtin_amdgcn_exp2f(X[i]); PIN(t_); X[i] = t_; } while (0)
#define CV(P, k, X, i) do { unsigned t_ = pk2(X[i], X[(i) + 1]); PIN(t_); P[k] = t_; } while (0)
#define BC8(x) __builtin_bit_cast(bf16x8, x)
#define UNIT(CUR, NXT, KN, QN, VP, OP, DO_A) UNIT_(CUR, NXT, KN, QN, VP, OP, DO_A, true, false)
#define UNIT_(CUR, NXT, KN, QN, VP, OP, DO_A, DO_P, FIRST) do { \
    if (DO_P) { OP = MFMA(VP[0], BC8(plo), (FIRST) ? zero : OP); SBAR(); CV(phi, 3, NXT, 14); SBAR(); } \
    EX(CUR, 0); EX(CUR, 1); SBAR(); \
    if (DO_P) { OP = MFMA(VP[1], BC8(phi), OP); SBAR(); } \
    EX(CUR, 2); EX(CUR, 3); CV(plo, 0, CUR, 0); SBAR(); \
    if (DO_A) { NXT = MFMA(KN[0], QN[0], zero); SBAR(); } \
    EX(CUR, 4); EX(CUR, 5); CV(plo, 1, CUR, 2); SBAR(); \
    if (DO_A) { NXT = MFMA(KN[1], QN[1], NXT); SBAR(); } \
    EX(CUR, 6); EX(CUR, 7); CV(plo, 2, CUR, 4); EX(CUR, 8); EX(CUR, 9); CV(plo, 3, CUR, 6); EX(CUR, 10); EX(CUR, 11); CV(phi, 0, CUR, 8); \
    EX(CUR, 12); EX(CUR, 13); CV(phi, 1, CUR, 10); EX(CUR, 14); EX(CUR, 15); CV(phi, 2, CUR, 12); SBAR(); \
  } while (0)

DI void a_attn(LAS char* lds, unsigned ldsb, char* ws, int head, int qt, int a, int lane, u64 tag) {
  const int hh = lane >> 5;
  const __amdgpu_buffer_rsrc_t rs = __builtin_amdgcn_make_buffer_rsrc(ws, 0, WS_END, 0x00020000);
  poll_flags<0>(ws, WS_FQ + (((lane < 4 ? 16 : lane < 8 ? 32 : 0) + head) * 64 + (lane < 8 ? 16 * a : 4 * qt) + (lane & 3)) * 128, tag);

  const unsigned qp = WS_QKV + (head * 64 + qt * 4) * 2048, vo = lane * 16;
  const unsigned kp = WS_QKV + ((16 + head) * 64 + a * 16) * 2048, vp = WS_QKV + ((32 + head) * 64 + a * 16) * 2048;
#define LDF(p, t, s) LD_DS(rs, vo + (s) * 1024, (p) + (t) * 2048)
  bf16x8 q0[2], q1[2], q2[2], q3[2], k0[2], k1[2], k2[2], k3[2], v0[2], v1[2], v2[2], v3[2];
  k0[0] = LDF(kp, 0, 0); k0[1] = LDF(kp, 0, 1); q0[0] = LDF(qp, (a + 1) & 3, 0); q0[1] = LDF(qp, (a + 1) & 3, 1); SBAR();
  q1[0] = LDF(qp, (a + 2) & 3, 0); q1[1] = LDF(qp, (a + 2) & 3, 1); SBAR();
  q2[0] = LDF(qp, (a + 3) & 3, 0); q2[1] = LDF(qp, (a + 3) & 3, 1); SBAR(); q3[0] = LDF(qp, (a + 4) & 3, 0); q3[1] = LDF(qp, (a + 4) & 3, 1); SBAR();
  v0[0] = LDF(vp, 0, 0); v0[1] = LDF(vp, 0, 1);
  k1[0] = LDF(kp, 1, 0); k1[1] = LDF(kp, 1, 1); v1[0] = LDF(vp, 1, 0); v1[1] = LDF(vp, 1, 1);
  k2[0] = LDF(kp, 2, 0); k2[1] = LDF(kp, 2, 1); v2[0] = LDF(vp, 2, 0); v2[1] = LDF(vp, 2, 1);
  k3[0] = LDF(kp, 3, 0); k3[1] = LDF(kp, 3, 1); v3[0] = LDF(vp, 3, 0); v3[1] = LDF(vp, 3, 1);
  const unsigned rest_off = WS_FQ + (((lane & 16 ? 32 : 16) + head) * 64 + 16 * a + (lane & 15)) * 128;
  const u64 rest = __hip_atomic_load((GAS u64*)(ws + rest_off), RLX_AGENT);
  SBAR();
  __builtin_amdgcn_s_setprio(3);
  const f32x16 zero = {};
  float mref[4];
  f32x16 o0, o1, o2, o3, sa, sb;
  o0 = MFMA(k0[0], q0[0], zero); o0 = MFMA(k0[1], q0[1], o0); o1 = MFMA(k0[0], q1[0], zero); o1 = MFMA(k0[1], q1[1], o1);
  o2 = MFMA(k0[0], q2[0], zero); o2 = MFMA(k0[1], q2[1], o2); o3 = MFMA(k0[0], q3[0], zero); o3 = MFMA(k0[1], q3[1], o3);
#define SETREF(s_, Q, i) do { \
    float m_ = fmaxf(fmaxf(s_[0], s_[1]), s_[2]), n_ = fmaxf(fmaxf(s_[3], s_[4]), s_[5]); \
    m_ = fmaxf(fmaxf(m_, s_[6]), s_[7]); n_ = fmaxf(fmaxf(n_, s_[8]), s_[9]); m_ = fmaxf(fmaxf(m_, s_[10]), s_[11]); n_ = fmaxf(fmaxf(n_, s_[12]), s_[13]); \
    m_ = fmaxf(fmaxf(m_, s_[14]), s_[15]); \
    m_ = xhalf_max(fmaxf(m_, n_)) + SHIFT; \
    const float h_ = rbf(m_), l_ = rbf(m_ - h_); mref[i] = h_ + l_; \
    u32x4 t_ = __builtin_bit_cast(u32x4, Q[1]); t_[2] = hh ? pk2(-h_, -l_) : t_[2]; Q[1] = __builtin_bit_cast(bf16x8, t_); } while (0)
  SETREF(o0, q0, 0);
  sa = MFMA(k0[0], q0[0], zero); sa = MFMA(k0[1], q0[1], sa);
  SETREF(o1, q1, 1); SETREF(o2, q2, 2); SETREF(o3, q3, 3);
  u32x4 plo, phi;
  SBAR();
  lds_signal(ldsb, C_AGO);
  __builtin_amdgcn_s_setprio(1);

#define TILE(KC, VC, VP, KN, T, PF, PFK, LAST) do { \
    UNIT(sa, sb, KC, q1, VP, o3, true); \
    if (PF) { VP[0] = LDF(vp, (T) + 3, 0); VP[1] = LDF(vp, (T) + 3, 1); SBAR(); } \
    UNIT(sb, sa, KC, q2, VC, o0, true); \
    UNIT(sa, sb, KC, q3, VC, o1, true); \
    if (PFK) { KC[0] = LDF(kp, (T) + 4, 0); KC[1] = LDF(kp, (T) + 4, 1); } SBAR(); \
    UNIT(sb, sa, KN, q0, VC, o2, !(LAST)); \
  } while (0)
  phi = (u32x4){};
  UNIT_(sa, sb, k0, q1, v0, o3, true, false, false);
  UNIT_(sb, sa, k0, q2, v0, o0, true, true, true);
  UNIT_(sa, sb, k0, q3, v0, o1, true, true, true);
  if (__builtin_expect(!__all(rest == tag), 0)) poll_flags<0>(ws, rest_off, tag);
  __builtin_amdgcn_fence(__ATOMIC_ACQUIRE, "wavefront"); asm volatile("" ::: "memory");
  k0[0] = LDF(kp, 4, 0); k0[1] = LDF(kp, 4, 1); SBAR();
  UNIT_(sb, sa, k1, q0, v0, o2, true, true, true);
  o3 = zero;
#pragma unroll 1
  for (int t = 1; t < 13; t += 4) {
    const unsigned kp_ = kp, vp_ = vp;
    { const unsigned kp = kp_ + t * 2048, vp = vp_ + t * 2048;
      TILE(k1, v1, v0, k2, 0, true, true, false);
      TILE(k2, v2, v1, k3, 1, true, true, false);
      TILE(k3, v3, v2, k0, 2, true, true, false);
      TILE(k0, v0, v3, k1, 3, true, t < 9, false);
    }
  }
  TILE(k1, v1, v0, k2, 13, false, false, false);
  TILE(k2, v2, v1, k3, 14, false, false, false);
#define PUTO(O, i) do { const int d_ = (a + 1 + (i)) & 3; \
    *(LAS float*)(lds + L_MST + (d_ * 4 + (i)) * 256 + lane * 4) = hh ? O[12] : mref[i];     \
    LAS char* p_ = lds + L_MRG + (d_ * 3 + (i)) * 4096 + lane * 16; \
    _Pragma("unroll") for (int g = 0; g < 4; ++g) *(LAS f32x4*)(p_ + g * 1024) = (f32x4){O[4 * g], O[4 * g + 1], O[4 * g + 2], O[4 * g + 3]}; SBAR(); } while (0)
  const unsigned gdone = CNT_LD(C_GDONE);
  UNIT(sa, sb, k3, q1, v2, o3, true);
  UNIT(sb, sa, k3, q2, v3, o0, true);
  CNT_CHECK(gdone, C_GDONE, 4);
  PUTO(o0, 0);
  UNIT(sa, sb, k3, q3, v3, o1, true);
  PUTO(o1, 1);
  UNIT(sb, sa, k0, q0, v3, o2, false);
  PUTO(o2, 2);
  lds_signal(ldsb, C_MRG);
  phi[3] = pk2(sb[14], sb[15]);
  o3 = MFMA(v3[0], BC8(plo), o3); o3 = MFMA(v3[1], BC8(phi), o3);

  lds_wait<0>(lds, C_MRG, 4);

  f32x16 o;
  bool bad = false;
  {
    float mw[4], lw[4]; f32x4 t[3][4];
#pragma unroll
    for (int i = 0; i < 3; ++i) { LAS const float* p = (LAS const float*)(lds + L_MST + (a * 4 + i) * 256) + (lane & 31); mw[i] = p[0]; lw[i] = p[32]; }
#pragma unroll
    for (int i = 0; i < 3; ++i)
#pragma unroll
      for (int g = 0; g < 4; ++g) t[i][g] = *(LAS const f32x4*)(lds + L_MRG + (a * 3 + i) * 4096 + lane * 16 + g * 1024);
    SBAR();
    mw[3] = mref[3]; lw[3] = hi_half(o3[12]);
    float M = -INFINITY, f[4];
#pragma unroll
    for (int i = 0; i < 4; ++i) { bad |= !(lw[i] > LMIN && lw[i] < 0x1p90f); M = fmaxf(M, mw[i] + __builtin_amdgcn_logf(lw[i])); }
#pragma unroll
    for (int i = 0; i < 4; ++i) f[i] = __builtin_amdgcn_exp2f(mw[i] - M);
    const float rl = __builtin_amdgcn_rcpf(fmaf(f[0], lw[0], f[1] * lw[1]) + fmaf(f[2], lw[2], f[3] * lw[3]));
#pragma unroll
    for (int i = 0; i < 4; ++i) f[i] *= rl;
    float chk[4] = {};
#pragma unroll
    for (int e = 0; e < 16; ++e) {
      o[e] = fmaf(t[2][e >> 2][e & 3], f[2], fmaf(t[1][e >> 2][e & 3], f[1], fmaf(t[0][e >> 2][e & 3], f[0], o3[e] * f[3])));
      chk[e & 3] += fabsf(o[e]);
    }
    bad |= !((chk[0] + chk[1]) + (chk[2] + chk[3]) < INFINITY);
  }
  if (__builtin_expect(__any(bad), 0)) {
    bf16x8 qa[2]; qa[0] = q3[0];
    u32x4 t_ = __builtin_bit_cast(u32x4, q3[1]); t_[2] = hh ? 0u : t_[2]; qa[1] = __builtin_bit_cast(bf16x8, t_);
    const unsigned kq = kp - a * 16 * 2048, vq = vp - a * 16 * 2048;
    float m = -INFINITY; o = zero;
#pragma unroll 1
    for (int t = 0; t < 64; ++t) {
      f32x16 s_ = MFMA(LDF(kq, t, 0), qa[0], zero); s_ = MFMA(LDF(kq, t, 1), qa[1], s_);
      float mx = s_[0];
#pragma unroll
      for (int e = 1; e < 16; ++e) mx = fmaxf(mx, s_[e]);
      const float mn = fmaxf(m, xhalf_max(mx));
      const float al = __builtin_amdgcn_exp2f(m - mn);
      m = mn;
#pragma unroll
      for (int e = 0; e < 16; ++e) { o[e] *= al; s_[e] = __builtin_amdgcn_exp2f(s_[e] - mn); }
      o = MFMA(LDF(vq, t, 0), __builtin_bit_cast(bf16x8, pack8(s_, 0)), o);
      o = MFMA(LDF(vq, t, 1), __builtin_bit_cast(bf16x8, pack8(s_, 1)), o);
    }
    const float rl = __builtin_amdgcn_rcpf(hi_half(o[12]));
#pragma unroll
    for (int e = 0; e < 16; ++e) o[e] *= rl;
  }
  {
    if (hh) { o[12] = 0.f; o[13] = 0.f; o[14] = 0.f; o[15] = 0.f; }
    const unsigned off = WS_O + ((qt * 4 + a) * 16 + head) * 2048 + lane * 16;
    ST_WT(pack8(o, 0), rs, off); ST_WT(pack8(o, 1), rs, off + 1024);
  }

  publish(ws, WS_FO + (qt * 16 + head) * FG + a * FW, tag);
  asm volatile(".Lfb_code_b:");
}

DI void g_phase2(LAS char* lds, unsigned ldsb, const char* x, const char* wo, const char* w2, char* ws, char* out, int rt64, int ct, int g, int lane, u64 tag) {
  const int hh = lane >> 5, blk = (lane >> 4) & 1, q4 = (lane & 15) >> 2, p4 = lane & 3;
  const int a0 = g >> 1, c0 = g & 1;
  const unsigned ring = L_G2 + g * 24576;
  const unsigned va = lane * 16;
  const unsigned vb0 = (lane >> 3) * XP + (((lane & 7) ^ (((lane >> 4) & 1) << 2)) * 16), vb1 = vb0 + 8 * XP;
  const char* hb0 = ws + WS_H + (size_t)((2 * rt64 + a0) * 56 + 14 * g) * 2048;
  const char* hb1 = ws + WS_H + (size_t)((2 * rt64 + (a0 ^ 1)) * 56 + 14 * g) * 2048;
  const char* wb = w2 + (size_t)(14 * g * 32) * XP + ct * 128;
  unsigned tb[2];
#pragma unroll
  for (int c = 0; c < 2; ++c) tb[c] = ldsb + (4 * hh + q4) * 128 + ((c ^ c0 ^ (q4 >> 1)) * 64) + blk * 32 + p4 * 8;
  bf16x8 ar[8][2];
#define LDA(dst, base) asm volatile("global_load_dwordx4 %0, %1, %2 sc1" : "=v"(dst) : "v"(va), "s"(base) : "memory")
  const int r2 = (7 * (rt64 & 3) + 4 * ct) % 28;
#define PST(st) ((st) + r2 >= 28 ? (st) + r2 - 28 : (st) + r2)
#define ISSUE_B(st) do { const char* s_ = wb + PST(st) * 16 * XP; glds(s_, vb0, ldsb + ring + ((st) % 12) * 2048); glds(s_, vb1, ldsb + ring + ((st) % 12) * 2048 + 1024); } while (0)
#define ISSUE_A(st) do { LDA(ar[(st) % 8][0], hb0 + PST(st) * 1024); LDA(ar[(st) % 8][1], hb1 + PST(st) * 1024); } while (0)
  const unsigned vw3 = (min(16 + 8 + (lane >> 3), DH - 1)) * XP + (((lane & 7) ^ (((lane >> 4) & 1) << 2)) * 16);
#define ISSUE_W(i) do { const char* b_ = wo + (size_t)((4 * g + (i)) * DH) * XP + ct * 128; const unsigned d_ = ldsb + ring + ((28 + 2 * (i)) % 12) * 2048; \
    glds(b_, vb0, d_); glds(b_, vb1, d_ + 1024); glds(b_ + 16 * XP, vb0, d_ + 2048); glds(b_, vw3, d_ + 3072); } while (0)
  typedef unsigned u32x2 __attribute__((ext_vector_type(2)));
  const size_t xo = (size_t)(rt64 * 64 + a0 * 32 + (lane & 31)) * XP + ct * 128 + c0 * 64;
  u32x2 xres[4];
#pragma unroll
  for (int q = 0; q < 4; ++q) xres[q] = *(const u32x2*)(x + xo + q * 16 + hh * 8);
  lds_wait(lds, C_GDONE, 4);
  ISSUE_B(0); ISSUE_B(1); ISSUE_B(2); ISSUE_B(3); ISSUE_B(4); ISSUE_B(5); ISSUE_B(6);
  poll_flags<2>(ws, WS_FH + ((rt64 >> 1) * 16 + 4 * g + (lane & 3)) * FG + (((2 * rt64) & 3) + ((lane >> 2) & 1)) * FW, tag);

  ISSUE_A(0); ISSUE_A(1); ISSUE_A(2); ISSUE_A(3); ISSUE_A(4); ISSUE_A(5); ISSUE_A(6);
  f32x16 acc[2][2] = {};
  const unsigned t0 = tb[0] + ring, t1 = tb[1] + ring;
  bf16x8 wq[2][2];
#define RD(st, N) do { asm volatile("s_waitcnt vmcnt(" #N ")" : "+v"(ar[(st) % 8][0]), "+v"(ar[(st) % 8][1]) :: "memory"); \
    wq[(st) & 1][0] = cat(trd_a(t0 + ((st) % 12) * 2048), trd_a(t0 + ((st) % 12) * 2048 + 1024)); \
    wq[(st) & 1][1] = cat(trd_a(t1 + ((st) % 12) * 2048), trd_a(t1 + ((st) % 12) * 2048 + 1024)); SBAR(); } while (0)
#define MM(st, i) acc[(i) >> 1][(i) & 1] = MFMA(wq[(st) & 1][(i) & 1], ar[(st) % 8][(i) >> 1], acc[(i) >> 1][(i) & 1])
#define FENCE() do { PIN(acc[0][0]); PIN(acc[0][1]); PIN(acc[1][0]); PIN(acc[1][1]); SBAR(); } while (0)
  RD(0, 12);
  MM(0, 0); MM(0, 1); MM(0, 2); FENCE(); RD(1, 10); ISSUE_B(7); ISSUE_A(7); SBAR();
  MM(0, 3); MM(1, 0); MM(1, 1); FENCE(); RD(2, 12); ISSUE_B(8); ISSUE_A(8); SBAR();
  MM(1, 2); MM(1, 3); MM(2, 0); FENCE(); ISSUE_B(9); ISSUE_A(9); SBAR();
  MM(2, 1); MM(2, 2); MM(2, 3); FENCE(); RD(3, 18); ISSUE_B(10); ISSUE_A(10); SBAR();
  MM(3, 0); MM(3, 1); MM(3, 2); FENCE(); RD(4, 20); SBAR();
  MM(3, 3); MM(4, 0); MM(4, 1); FENCE(); RD(5, 18); ISSUE_B(11); ISSUE_A(11); SBAR();
  MM(4, 2); MM(4, 3); MM(5, 0); FENCE(); ISSUE_B(12); ISSUE_A(12); SBAR();
  MM(5, 1); MM(5, 2); MM(5, 3); FENCE(); RD(6, 24); ISSUE_B(13); ISSUE_A(13); SBAR();
  MM(6, 0); MM(6, 1); MM(6, 2); FENCE(); RD(7, 24); SBAR();
  MM(6, 3); MM(7, 0); MM(7, 1); FENCE(); RD(8, 20); ISSUE_B(14); ISSUE_A(14); SBAR();
 MM(7, 2); MM(7, 3); MM(8, 0); FENCE(); ISSUE_B(15); ISSUE_A(15); SBAR();
  MM(8, 1); MM(8, 2); MM(8, 3); FENCE(); RD(9, 24); ISSUE_B(16); ISSUE_A(16); SBAR();
  MM(9, 0); MM(9, 1); MM(9, 2); FENCE(); RD(10, 24); SBAR();
  MM(9, 3); MM(10, 0); MM(10, 1); FENCE(); RD(11, 20); ISSUE_B(17); ISSUE_A(17); SBAR();
  MM(10, 2); MM(10, 3); MM(11, 0); FENCE(); ISSUE_B(18); ISSUE_A(18); SBAR();
  MM(11, 1); MM(11, 2); MM(11, 3); FENCE(); RD(12, 24); ISSUE_B(19); ISSUE_A(19); SBAR();
  MM(12, 0); MM(12, 1); MM(12, 2); FENCE(); RD(13, 24); SBAR();
  MM(12, 3); MM(13, 0); MM(13, 1); FENCE(); RD(14, 20); ISSUE_B(20); ISSUE_A(20); SBAR();
  MM(13, 2); MM(13, 3); MM(14, 0); FENCE(); ISSUE_B(21); ISSUE_A(21); SBAR();
  MM(14, 1); MM(14, 2); MM(14, 3); FENCE(); RD(15, 24); ISSUE_B(22); ISSUE_A(22); SBAR();
 MM(15, 0); MM(15, 1); MM(15, 2); FENCE(); RD(16, 24); SBAR();
  MM(15, 3); MM(16, 0); MM(16, 1); FENCE(); RD(17, 20); ISSUE_B(23); ISSUE_A(23); SBAR();
  MM(16, 2); MM(16, 3); MM(17, 0); FENCE(); ISSUE_B(24); ISSUE_A(24); SBAR();
  MM(17, 1); MM(17, 2); MM(17, 3); FENCE(); RD(18, 24); ISSUE_B(25); ISSUE_A(25); SBAR();
  MM(18, 0); MM(18, 1); MM(18, 2); FENCE(); RD(19, 24); SBAR();
  MM(18, 3); MM(19, 0); MM(19, 1); FENCE(); RD(20, 20); ISSUE_B(26); ISSUE_A(26); SBAR();
  MM(19, 2); MM(19, 3); MM(20, 0); FENCE(); ISSUE_B(27); ISSUE_A(27); SBAR();
  MM(20, 1); MM(20, 2); MM(20, 3); FENCE(); RD(21, 24); ISSUE_W(0); SBAR();
  MM(21, 0); MM(21, 1); MM(21, 2); FENCE(); RD(22, 24); SBAR();
  MM(21, 3); MM(22, 0); MM(22, 1); FENCE(); RD(23, 20); ISSUE_W(1); SBAR();
  MM(22, 2); MM(22, 3); MM(23, 0); FENCE(); ISSUE_W(2); SBAR();
  MM(23, 1); MM(23, 2); MM(23, 3); FENCE(); RD(24, 24); ISSUE_W(3); SBAR();
  MM(24, 0); MM(24, 1); MM(24, 2); FENCE(); RD(25, 24); SBAR();
  MM(24, 3); MM(25, 0); MM(25, 1); FENCE(); RD(26, 20); SBAR();
  MM(25, 2); MM(25, 3); MM(26, 0); FENCE(); SBAR();
  MM(26, 1); MM(26, 2); MM(26, 3); FENCE(); RD(27, 16); SBAR();
  MM(27, 0); MM(27, 1); MM(27, 2); FENCE(); SBAR();
  MM(27, 3); FENCE(); SBAR();

  const unsigned fo = WS_FO + ((rt64 >> 1) * 16 + 4 * g + (lane & 3)) * FG + (((2 * rt64) & 3) + ((lane >> 2) & 1)) * FW;
  const u64 seen = __hip_atomic_load((GAS u64*)(ws + fo), RLX_AGENT);
  WAITV(0);
  bf16x8 wf[8][2];
#pragma unroll
  for (int k = 0; k < 8; ++k) { wf[k][0] = cat(trd_a(t0 + 8192 + k * 2048), trd_a(t0 + 8192 + k * 2048 + 1024)); wf[k][1] = cat(trd_a(t1 + 8192 + k * 2048), trd_a(t1 + 8192 + k * 2048 + 1024)); }
#pragma unroll
  for (int q = 0; q < 4; ++q) {
    acc[0][0][4 * q] += __uint_as_float(xres[q][0] << 16); acc[0][0][4 * q + 1] += __uint_as_float(xres[q][0] & 0xffff0000u);
    acc[0][0][4 * q + 2] += __uint_as_float(xres[q][1] << 16); acc[0][0][4 * q + 3] += __uint_as_float(xres[q][1] & 0xffff0000u);
  }
  PIN(acc[0][0]);

  if (!__all(seen == tag)) poll_flags<0>(ws, fo, tag);
  __builtin_amdgcn_fence(__ATOMIC_ACQUIRE, "wavefront"); asm volatile("" ::: "memory");

  {
    const __amdgpu_buffer_rsrc_t rs = __builtin_amdgcn_make_buffer_rsrc(ws, 0, WS_END, 0x00020000);
    const unsigned ob0 = WS_O + ((2 * rt64 + a0) * 16 + 4 * g) * 2048, ob1 = WS_O + ((2 * rt64 + (a0 ^ 1)) * 16 + 4 * g) * 2048;
    bf16x8 of[2][8];
#pragma unroll
    for (int k = 0; k < 8; ++k) { of[0][k] = LD_DS(rs, va + (k & 3) * 1024, ob0 + (k >> 2) * 4096); of[1][k] = LD_DS(rs, va + (k & 3) * 1024, ob1 + (k >> 2) * 4096); SBAR(); }
#pragma unroll
    for (int k = 0; k < 8; ++k)
#pragma unroll
      for (int a = 1; a >= 0; --a)
#pragma unroll
        for (int c = 1; c >= 0; --c) acc[a][c] = MFMA(wf[k][c], of[a][k], acc[a][c]);
  }
#define PUT16(V, off) do { _Pragma("unroll") for (int q = 0; q < 4; ++q) *(LAS f32x4*)(lds + ring + (off) + q * 1024 + va) = (f32x4){V[4 * q], V[4 * q + 1], V[4 * q + 2], V[4 * q + 3]}; } while (0)
  PUT16(acc[1][1], 8192); PUT16(acc[1][0], 4096); PUT16(acc[0][1], 0);
  lds_signal(ldsb, C_RED);
  lds_wait<0>(lds, C_RED, 4);

  f32x16 y = acc[0][0];
  {
    f32x4 t[3][4];
#pragma unroll
    for (int d = 1; d < 4; ++d)
#pragma unroll
      for (int q = 0; q < 4; ++q) t[d - 1][q] = *(LAS const f32x4*)(lds + L_G2 + (g ^ d) * 24576 + (d - 1) * 4096 + q * 1024 + va);
#pragma unroll
    for (int e = 0; e < 16; ++e) y[e] = (y[e] + t[0][e >> 2][e & 3]) + (t[1][e >> 2][e & 3] + t[2][e >> 2][e & 3]);
  }
#pragma unroll
  for (int q = 0; q < 4; q += 2) {
    const auto r0 = __builtin_amdgcn_permlane32_swap(pk2(y[4 * q], y[4 * q + 1]), pk2(y[4 * q + 4], y[4 * q + 5]), false, false);
    const auto r1 = __builtin_amdgcn_permlane32_swap(pk2(y[4 * q + 2], y[4 * q + 3]), pk2(y[4 * q + 6], y[4 * q + 7]), false, false);
    *(u32x4*)(out + xo + (q + hh) * 16) = (u32x4){r0[0], r1[0], r0[1], r1[1]};
  }
}

__global__ void __launch_bounds__(512, 2) fused_block(const char* __restrict__ x, const char* __restrict__ wqkv, const char* __restrict__ wo,
                                                      const char* __restrict__ w1, const char* __restrict__ w2, char* out, char* ws) {
  u64 pc0; unsigned clen;
  asm volatile("s_getpc_b64 %0\n.Lfb_code_a:\n\ts_mov_b32 %1, .Lfb_code_b-.Lfb_code_a" : "=s"(pc0), "=s"(clen));
  extern __shared__ __attribute__((aligned(16))) char smem[];
  LAS char* lds = (LAS char*)smem;
  const unsigned ldsb = (unsigned)(uintptr_t)lds;
  const int lane = threadIdx.x & 63, wave = __builtin_amdgcn_readfirstlane(threadIdx.x >> 6);
  const int xcd = blockIdx.x & 7, slot = blockIdx.x >> 3;

  u64 epoch;
  constexpr int RPX = 4;
  const int rt = RPX * (xcd % (16 / RPX)) + (slot % RPX), j = (32 / RPX) * (xcd / (16 / RPX)) + slot / RPX;
  const int rot = (j + 2 * (rt & 3)) % 7;
  if (__builtin_expect(wave < 4, 1)) {
    g_phase1(lds, ldsb, x, ws, rt, j, rot, wave, lane, epoch);
    const u64 tag = epoch + 1;
    int lane2 = lane; OPAQUE(lane2);
    if (slot < 28) g_phase2(lds, ldsb, x, wo, w2, ws, out, 4 * xcd + slot / 7, slot % 7, wave, lane2, tag);
    else if (blockIdx.x == 255 && wave == 0) {
#pragma unroll
      for (int i = 0; i < 4; ++i) poll_flags(ws, WS_FQ + (3 * (i * 4 + (lane >> 4)) * 64 + (lane & 15) * 4) * 128, tag);
      if (lane == 0) __hip_atomic_store((GAS u64*)(ws + WS_EPOCH), tag, RLX_AGENT);
    }
  } else {
    unsigned pfc = 0;
    if (wave == 7) {
      const unsigned off = min((unsigned)((slot & 7) * 64 + lane) * 128u, clen);
      asm volatile("global_load_dword %0, %1, %2" : "+v"(pfc) : "v"(off), "s"(pc0) : "memory"); }
    a_phase1(lds, ldsb, ws, wqkv, w1, j, rot, wave - 4, lane, epoch, pfc);

    a_attn(lds, ldsb, ws, 2 * xcd + (slot >> 4), slot & 15, wave - 4, lane, epoch + 1);
  }
}

extern "C" void kernel_launch(void* const* d_in, const int* in_sizes, int n_in, void* d_out, int out_size, void* d_ws, size_t ws_size, hipStream_t stream) {
  (void)in_sizes; (void)n_in; (void)out_size; (void)ws_size;
  (void)hipFuncSetAttribute((const void*)fused_block, hipFuncAttributeMaxDynamicSharedMemorySize, LDS_BYTES);
  fused_block<<<256, 512, LDS_BYTES, stream>>>((const char*)d_in[0], (const char*)d_in[1], (const char*)d_in[2], (const char*)d_in[4], (const char*)d_in[5], (char*)d_out, (char*)d_ws);
}
```
